```python
import torch

def _softmax(x, axis=-1):
    return torch.softmax(x, dim=axis)
def _gelu(x):
    return torch.nn.functional.gelu(x, approximate="tanh")
def _silu(x):
    return torch.nn.functional.silu(x)
def _sigmoid(x):
    return torch.sigmoid(x)
def _layernorm(x, axis=-1, eps=1e-5):
    m = x.mean(dim=axis, keepdim=True)
    v = x.var(dim=axis, keepdim=True, unbiased=False)
    return (x - m) / torch.sqrt(v + eps)
def _scatter_add(tgt, idx, src, axis=0):
    return tgt.clone().index_add_(axis, idx, src)
def _segment_sum(src, seg, n):
    out = torch.zeros((n,) + src.shape[1:], dtype=src.dtype)
    return out.index_add_(0, seg, src)
def _pad(x, pw):
    flat = tuple(v for pair in reversed(pw) for v in pair)
    return torch.nn.functional.pad(x, flat)

kernel_name = 'sequential_preLN__sliding_block__dense_activation_squared_relu__prompt'

def setup_inputs():

    _w = (lambda a: (lambda t: t.to(torch.bfloat16) if t.is_floating_point() else t)(torch.as_tensor(a)))
    def _A_setup():
        B, S, D = 1, 2048, 512
        return {
            "x": _w((lambda *s: torch.randn(*s, dtype=torch.bfloat16) * 0.05)(B, S, D)),
            "wqkv": _w((lambda *s: torch.randn(*s, dtype=torch.bfloat16) * 0.05)(D, 3 * D)),
            "wo": _w((lambda *s: torch.randn(*s, dtype=torch.bfloat16) * 0.05)(D, D)),
        }
    _A_in = _A_setup()

    def _B_setup():
        B, S, D, F = 1, 2048, 512, 1376
        return {
            "x": _w((lambda *s: torch.randn(*s, dtype=torch.bfloat16) * 0.05)(B, S, D)),
            "w1": _w((lambda *s: torch.randn(*s, dtype=torch.bfloat16) * 0.05)(D, F)),
            "b1": _w((lambda *s: torch.randn(*s, dtype=torch.bfloat16) * 0.4)(F)),
            "w2": _w((lambda *s: torch.randn(*s, dtype=torch.bfloat16) * 0.05)(F, D)),
            "b2": _w((lambda *s: torch.randn(*s, dtype=torch.bfloat16) * 1.6)(D)),
        }
    _B_in = _B_setup()

    return {
        "A_x": _A_in["x"],
        "A_wqkv": _A_in["wqkv"],
        "A_wo": _A_in["wo"],
        "B_x": _B_in["x"],
        "B_w1": _B_in["w1"],
        "B_b1": _B_in["b1"],
        "B_w2": _B_in["w2"],
        "B_b2": _B_in["b2"],
    }

def reference(A_x, A_wqkv, A_wo, B_x, B_w1, B_b1, B_w2, B_b2):
    def _A_ref(x, wqkv, wo):
        B, S, D = x.shape
        H = 32; Dh = D // H; W = 256
        qkv = (lambda x, *d: x.permute(*d))((x @ wqkv).reshape(B, S, 3, H, Dh), 2, 0, 3, 1, 4)
        q, k, v = qkv[0], qkv[1], qkv[2]
        scores = torch.einsum("bhqd,bhkd->bhqk", q, k) * (Dh ** -0.5)
        idx = torch.arange(S)
        mask = (idx[:, None] - idx[None, :] >= 0) & (idx[:, None] - idx[None, :] < W)
        scores = torch.where(mask[None, None], scores, -1e9)
        p = _softmax(scores, axis=-1)
        o = torch.einsum("bhqk,bhkd->bhqd", p, v)
        return (lambda x, *d: x.permute(*d))(o, 0, 2, 1, 3).reshape(B, S, D) @ wo

    def _B_ref(x, w1, b1, w2, b2):
        h = torch.einsum("bsd,df->bsf", x, w1) + b1
        zero = torch.zeros_like(h)
        r = torch.where(h > zero, h, zero)
        h = r * r
        out = torch.einsum("bsf,fd->bsd", h, w2) + b2
        return out

    x = A_x
    h = x + _A_ref(_layernorm(x, axis=-1), A_wqkv, A_wo)
    h2 = h + _B_ref(_layernorm(h, axis=-1), B_w1, B_b1, B_w2, B_b2)
    return h2
```

```cpp
#include <hip/hip_runtime.h>
#include <hip/hip_bf16.h>
typedef unsigned short u16;
__device__ __forceinline__ float bf2f(u16 b){ return __uint_as_float(((unsigned)b)<<16); }
__device__ __forceinline__ u16 f2bf(float f){ unsigned u=__float_as_uint(f); u += 0x7fffu + ((u>>16)&1u); return (u16)(u>>16); }
constexpr int S=2048, D=512, F=1376, H=32, DH=16, W=256;

__global__ void ln_k(const u16* x, u16* y){
  int r=blockIdx.x; int t=threadIdx.x;
  float v[8]; float s=0;
  for(int i=0;i<8;i++){ v[i]=bf2f(x[r*D+t*8+i]); s+=v[i]; }
  for(int m=32;m>=1;m>>=1) s+=__shfl_xor(s,m);
  float mean=s/D; float q=0;
  for(int i=0;i<8;i++){ float d=v[i]-mean; q+=d*d; }
  for(int m=32;m>=1;m>>=1) q+=__shfl_xor(q,m);
  float rstd=rsqrtf(q/D+1e-5f);
  for(int i=0;i<8;i++) y[r*D+t*8+i]=f2bf((v[i]-mean)*rstd);
}
template<int MODE>
__global__ void gemm_k(const u16* A,const u16* B,u16* C,int N,int K,const u16* bias,const u16* res){
  __shared__ float As[16][17], Bs[16][17];
  int tx=threadIdx.x&15, ty=threadIdx.x>>4;
  int row=blockIdx.y*16+ty, col=blockIdx.x*16+tx;
  float acc=0;
  for(int k0=0;k0<K;k0+=16){
    As[ty][tx]=bf2f(A[row*K+k0+tx]);
    Bs[ty][tx]=bf2f(B[(k0+ty)*N+col]);
    __syncthreads();
    for(int k=0;k<16;k++) acc+=As[ty][k]*Bs[k][tx];
    __syncthreads();
  }
  if(MODE==1) acc+=bf2f(res[row*N+col]);
  if(MODE==2){ acc+=bf2f(bias[col]); acc=acc>0?acc*acc:0; }
  if(MODE==3){ acc+=bf2f(bias[col])+bf2f(res[row*N+col]); }
  C[row*N+col]=f2bf(acc);
}
__global__ void attn_k(const u16* qkv,u16* o){
  int q=blockIdx.x*64+threadIdx.x; int h=blockIdx.y;
  float qv[DH]; for(int d=0;d<DH;d++) qv[d]=bf2f(qkv[q*1536+h*DH+d])*0.25f;
  float m=-1e30f,l=0,acc[DH]; for(int d=0;d<DH;d++) acc[d]=0;
  int k0=q-W+1; if(k0<0)k0=0;
  for(int k=k0;k<=q;k++){
    float s=0; for(int d=0;d<DH;d++) s+=qv[d]*bf2f(qkv[k*1536+512+h*DH+d]);
    float mn=fmaxf(m,s); float a=__expf(m-mn), p=__expf(s-mn);
    l=l*a+p; for(int d=0;d<DH;d++) acc[d]=acc[d]*a+p*bf2f(qkv[k*1536+1024+h*DH+d]);
    m=mn;
  }
  for(int d=0;d<DH;d++) o[q*D+h*DH+d]=f2bf(acc[d]/l);
}
extern "C" void kernel_launch(void* const* d_in, const int* in_sizes, int n_in, void* d_out, int out_size, void* d_ws, size_t ws_size, hipStream_t stream){
  const u16* x=(const u16*)d_in[0]; const u16* wqkv=(const u16*)d_in[1]; const u16* wo=(const u16*)d_in[2];
  const u16* w1=(const u16*)d_in[4]; const u16* b1=(const u16*)d_in[5]; const u16* w2=(const u16*)d_in[6]; const u16* b2=(const u16*)d_in[7];
  u16* ws=(u16*)d_ws; u16* xn=ws; u16* qkv=xn+S*D; u16* o=qkv+S*1536; u16* h=o+S*D; u16* hn=h+S*D; u16* h1=hn+S*D;
  ln_k<<<S,64,0,stream>>>(x,xn);
  gemm_k<0><<<dim3(1536/16,S/16),256,0,stream>>>(xn,wqkv,qkv,1536,D,nullptr,nullptr);
  attn_k<<<dim3(S/64,H),64,0,stream>>>(qkv,o);
  gemm_k<1><<<dim3(D/16,S/16),256,0,stream>>>(o,wo,h,D,D,nullptr,x);
  ln_k<<<S,64,0,stream>>>(h,hn);
  gemm_k<2><<<dim3(F/16,S/16),256,0,stream>>>(hn,w1,h1,F,D,b1,nullptr);
  gemm_k<3><<<dim3(D/16,S/16),256,0,stream>>>(h1,w2,(u16*)d_out,D,F,b2,h);
}
```

```cpp
#include <hip/hip_runtime.h>
#include <hip/hip_bf16.h>

#define STAMPS 0

namespace fb {
typedef unsigned short u16;
typedef unsigned long long u64;
typedef unsigned v4u __attribute__((ext_vector_type(4)));
typedef unsigned v2u __attribute__((ext_vector_type(2)));
typedef float f32x2 __attribute__((ext_vector_type(2)));
typedef float f32x4 __attribute__((ext_vector_type(4)));
typedef float f32x16 __attribute__((ext_vector_type(16)));
typedef __bf16 bf16x8 __attribute__((ext_vector_type(8)));
typedef __bf16 bf16x2 __attribute__((ext_vector_type(2)));
typedef short s16x4 __attribute__((ext_vector_type(4)));
#define LAS __attribute__((address_space(3)))
#define GAS __attribute__((address_space(1)))
typedef LAS char* lptr;
typedef __amdgpu_buffer_rsrc_t rsrc_t;
#define DI __device__ __forceinline__
#define RLX_AGENT __ATOMIC_RELAXED, __HIP_MEMORY_SCOPE_AGENT

constexpr int SEQ = 2048, DM = 512, FF = 1376;
constexpr float EPS = 1e-5f;
constexpr float QSCALE = 0.25f * 1.4426950408889634f;
constexpr float NEGBIG = -1e30f;
constexpr int SC1 = 16;

constexpr int WS_FLAG = 4096;
constexpr int WS_KV = 1 << 18;
constexpr int WS_O = WS_KV + (4 << 20);
constexpr int WS_H = WS_O + (2 << 20);
constexpr int WS_HST = WS_H + (2 << 20);
constexpr int WS_H1 = WS_HST + (1 << 18);
constexpr int WS_END = WS_H1 + 64 * 86 * 1024;
constexpr int WS_STAMP = 16 << 20;

constexpr int L_X = 0, L_RING = 65536, L_KV = 65536, L_WO = 0, L_W1 = 49152, L_MISC = 147456;
constexpr int L_ST1 = L_MISC, L_R1 = L_MISC + 512, L_ONES = L_MISC + 1024, L_Q1 = L_MISC + 4096;
constexpr int L_XCH = L_MISC, L_ST2 = L_MISC, L_CS2 = L_MISC + 1024;
constexpr int LDS_BYTES = 163840;

#define BAR() asm volatile("s_waitcnt lgkmcnt(0)\n\ts_barrier" ::: "memory")
#define DRAIN() asm volatile("s_waitcnt vmcnt(0)" ::: "memory")
#define SBAR() __builtin_amdgcn_sched_barrier(0)
#define STAMP(i) do {} while (0)
#define STAMP_OUT() do {} while (0)

DI f32x16 mfma32(v4u a, v4u b, f32x16 c) {
  return __builtin_amdgcn_mfma_f32_32x32x16_bf16(__builtin_bit_cast(bf16x8, a), __builtin_bit_cast(bf16x8, b), c, 0, 0, 0);
}
DI f32x4 mfma16(v4u a, v4u b, f32x4 c) {
  return __builtin_amdgcn_mfma_f32_16x16x32_bf16(__builtin_bit_cast(bf16x8, a), __builtin_bit_cast(bf16x8, b), c, 0, 0, 0);
}
DI unsigned pk2(float lo, float hi) {
  f32x2 v = {lo, hi};
  return __builtin_bit_cast(unsigned, __builtin_convertvector(v, bf16x2));
}
DI float bflo(unsigned u) { return __builtin_bit_cast(float, u << 16); }
DI float bfhi(unsigned u) { return __builtin_bit_cast(float, u & 0xffff0000u); }
DI v4u lds128(lptr p) { return *(LAS v4u*)p; }
DI void sts128(lptr p, v4u v) { *(LAS v4u*)p = v; }
DI v4u trfrag(lptr p, int o0, int o1) {
  typedef short v4s __attribute__((ext_vector_type(4)));
  v4s a = __builtin_amdgcn_ds_read_tr16_b64_v4i16((LAS v4s*)(p + o0));
  v4s b = __builtin_amdgcn_ds_read_tr16_b64_v4i16((LAS v4s*)(p + o1));
  v2u x = __builtin_bit_cast(v2u, a), y = __builtin_bit_cast(v2u, b);
  return v4u{x.x, x.y, y.x, y.y};
}
DI rsrc_t mkrsrc(const void* p, int bytes) { return __builtin_amdgcn_make_buffer_rsrc((void*)p, 0, bytes, 0x00020000); }
DI v4u gld(rsrc_t r, int voff, int soff) { return __builtin_amdgcn_raw_buffer_load_b128(r, voff, soff, 0); }
DI v4u gld1(rsrc_t r, int voff, int soff) { return __builtin_amdgcn_raw_buffer_load_b128(r, voff, soff, SC1); }
DI void gst1(rsrc_t r, v4u v, int voff, int soff) { __builtin_amdgcn_raw_buffer_store_b128(v, r, voff, soff, SC1); }
template <int B> DI v4u pack8(const f32x16& a) {
  return v4u{pk2(a[B], a[B + 1]), pk2(a[B + 2], a[B + 3]), pk2(a[B + 4], a[B + 5]), pk2(a[B + 6], a[B + 7])};
}
DI float xhalf_sum(float v) {
  auto r = __builtin_amdgcn_permlane32_swap(__builtin_bit_cast(unsigned, v), __builtin_bit_cast(unsigned, v), false, false);
  return __uint_as_float(r[0]) + __uint_as_float(r[1]);
}
DI float xhalf_max(float v) {
  auto r = __builtin_amdgcn_permlane32_swap(__builtin_bit_cast(unsigned, v), __builtin_bit_cast(unsigned, v), false, false);
  return fmaxf(__uint_as_float(r[0]), __uint_as_float(r[1]));
}
DI void split2(float v, float& hi, float& lo) { hi = bflo(pk2(v, 0.f) & 0xffffu); lo = v - hi; }

DI void wait_flags(GAS u64* p, u64 tag) {
  for (int sp = 0; sp < (1 << 20); ++sp) {
    const u64 f = __hip_atomic_load(p, RLX_AGENT);
    if (__all(f == tag)) break;
    __builtin_amdgcn_s_sleep(1);
  }
  asm volatile("" ::: "memory");
}

DI int crow(int r, int h) { return (r & 3) + 8 * (r >> 2) + 4 * h; }

template <int MODE>
DI void att_tile(lptr lds, int kaddr, int vaddr, v4u q, f32x16& o, int t, int h) {
  const v4u kf = lds128(lds + kaddr);
  const v4u v0 = lds128(lds + vaddr), v1 = lds128(lds + vaddr + 1024);
  f32x16 c;
#pragma unroll
  for (int r = 0; r < 16; ++r) c[r] = MODE == 0 ? 0.f : (MODE == 1 ? (crow(r, h) <= t ? 0.f : NEGBIG) : (crow(r, h) > t ? 0.f : NEGBIG));
  f32x16 s = mfma32(kf, q, c);
#pragma unroll
  for (int r = 0; r < 16; ++r) s[r] = __builtin_amdgcn_exp2f(s[r]);
  o = mfma32(v0, pack8<0>(s), o);
  o = mfma32(v1, pack8<8>(s), o);
}
DI f32x16 att_slow(lptr lds, int kaddr, int vaddr, int vstep, v4u q, int ntile, int t, int h) {
  float mx = NEGBIG;
#pragma unroll 1
  for (int i = 0; i < ntile; ++i) {
    f32x16 c;
#pragma unroll
    for (int r = 0; r < 16; ++r) c[r] = 0.f;
    const f32x16 s = mfma32(lds128(lds + kaddr - i * 8192), q, c);
#pragma unroll
    for (int r = 0; r < 16; ++r) {
      const bool ok = i == 0 ? crow(r, h) <= t : (i == 8 ? crow(r, h) > t : true);
      mx = fmaxf(mx, ok ? s[r] : NEGBIG);
    }
  }
  mx = xhalf_max(mx);
  f32x16 o;
#pragma unroll
  for (int r = 0; r < 16; ++r) o[r] = 0.f;
#pragma unroll 1
  for (int i = 0; i < ntile; ++i) {
    f32x16 c;
#pragma unroll
    for (int r = 0; r < 16; ++r) c[r] = 0.f;
    f32x16 s = mfma32(lds128(lds + kaddr - i * 8192), q, c);
#pragma unroll
    for (int r = 0; r < 16; ++r) {
      const bool ok = i == 0 ? crow(r, h) <= t : (i == 8 ? crow(r, h) > t : true);
      s[r] = ok ? __builtin_amdgcn_exp2f(s[r] - mx) : 0.f;
    }
    o = mfma32(lds128(lds + vaddr - i * vstep), pack8<0>(s), o);
    o = mfma32(lds128(lds + vaddr - i * vstep + 1024), pack8<8>(s), o);
  }
  return o;
}
template <int SH> struct Att {
  f32x16 o;
  int kaddr, vaddr, vstep, qt, m, t, h;
  v4u q;
  DI void init(int lane, int m_, int p, int tb, v4u q_) {
    m = m_; q = q_; t = lane & 31; h = lane >> 5; qt = 2 * tb + m;
    const int base = L_KV + (8 + m) * 8192 + p * 4096 + lane * 16;
    kaddr = base + SH * 1024;
    const bool mine = ((lane >> 4) & 1) == SH;
    vaddr = mine ? base + 2048 : L_ONES + lane * 16;
    vstep = mine ? 8192 : 0;
#pragma unroll
    for (int r = 0; r < 16; ++r) o[r] = 0.f;
  }
  DI void own(lptr lds) {
    att_tile<1>(lds, kaddr, vaddr, q, o, t, h);
    if (m) att_tile<0>(lds, kaddr - 8192, vaddr - vstep, q, o, t, h);
  }
  DI v4u rest(lptr lds) {
    const int nfull = qt < 7 ? qt : 7;
#pragma unroll 1
    for (int i = 1 + m; i <= nfull; ++i) att_tile<0>(lds, kaddr - i * 8192, vaddr - i * vstep, q, o, t, h);
    if (qt >= 8) att_tile<2>(lds, kaddr - 8 * 8192, vaddr - 8 * vstep, q, o, t, h);
    float l = o[8 - 8 * SH];
    float amax = 0.f;
#pragma unroll
    for (int r = 0; r < 8; ++r) amax = fmaxf(amax, fabsf(o[8 * SH + r]));
    const bool safe = (l > 0x1p-60f) & (l < 0x1p60f) & (amax < 0x1p120f);
    if (!__all(safe)) {
      o = att_slow(lds, kaddr, vaddr, vstep, q, (qt < 8 ? qt : 8) + 1, t, h);
      l = o[8 - 8 * SH];
    }
    const float inv = 1.f / l;
#pragma unroll
    for (int r = 0; r < 8; ++r) o[8 * SH + r] *= inv;
    return pack8<8 * SH>(o);
  }
};

DI int w2_lds(int kh, int u) { return u < 12 ? kh * 24576 + u * 2048 : (u < 36 ? 49152 + kh * 49152 + (u - 12) * 2048 : kh * 24576 + (u - 36) * 2048); }
DI int rot43(int u, int rot) { const int v = u + rot; return v >= 43 ? v - 43 : v; }

__global__ void __launch_bounds__(512, 2)
fused_block(const u16* __restrict__ x, const u16* __restrict__ wqkv, const u16* __restrict__ wo, const u16* __restrict__ w1,
            const u16* __restrict__ b1, const u16* __restrict__ w2, const u16* __restrict__ b2, u16* __restrict__ out, char* ws) {
  extern __shared__ __attribute__((aligned(16))) char smem[];
  const lptr lds = (lptr)smem;
  const int lane = threadIdx.x & 63, W = __builtin_amdgcn_readfirstlane(threadIdx.x >> 6);
  const int b = blockIdx.x, tb = (b & 7) * 4 + (b >> 6), n = (b >> 3) & 7, L = tb * 8 + n;
  const int rot = (n + 2 * (tb & 3)) & 7;
  const int cl = tb >> 1, j = (tb & 1) * 8 + n;
  const int f0 = j < 11 ? 3 * j : 11 + 2 * j, nt = j < 11 ? 3 : 2;
  const int rotE = n * 5;
  const rsrc_t rws = mkrsrc(ws, WS_END);
  GAS u64* flags = (GAS u64*)(ws + WS_FLAG);
  const int t = lane & 31, h = lane >> 5, l8 = lane >> 3, c8 = lane & 7;
  const int q4 = (lane & 15) >> 2, trc = ((lane >> 4) & 1) * 32 + (lane & 3) * 8;
  const int wst = l8 * 128 + ((c8 * 16) ^ (((l8 >> 1) & 1) * 64));
  STAMP(0);

  if (W >= 4) {
    const int hw = W - 4;
    const rsrc_t rx = mkrsrc(x, SEQ * DM * 2), rq = mkrsrc(wqkv, DM * 3 * DM * 2);
    const int xv = (tb * 64 + 16 * hw + l8) * 1024 + c8 * 16;
    const int wv = (16 * hw + l8) * 3072 + n * 128 + c8 * 16;
    v4u xr[8][2], wr[8][4], wq[8][2];
#pragma unroll
    for (int c = 0; c < 8; ++c) {
      const int cc = (c + rot) & 7;
      xr[c][0] = gld(rx, xv, cc * 128);
      xr[c][1] = gld(rx, xv + 8192, cc * 128);
      wr[c][0] = gld(rq, wv + 1024, cc * 196608);
      wr[c][1] = gld(rq, wv + 1024 + 24576, cc * 196608);
      wr[c][2] = gld(rq, wv + 2048, cc * 196608);
      wr[c][3] = gld(rq, wv + 2048 + 24576, cc * 196608);
    }
    const u64 tag = __hip_atomic_load((GAS u64*)ws, RLX_AGENT) * 6364136223846793005ull + 1442695040888963407ull;
    SBAR();
    if (hw < 2) sts128(lds + L_ONES + hw * 1024 + lane * 16, v4u{0x3f803f80u, 0x3f803f80u, 0x3f803f80u, 0x3f803f80u});
    const int xs = L_X + (hw >> 1) * 32768 + (c8 >> 1) * 1024 + (((c8 & 1) * 32 + (hw & 1) * 16 + l8) ^ c8) * 16;
    const int wsb = L_RING + hw * 2048 + wst;
    f32x2 sm[2] = {{0.f, 0.f}, {0.f, 0.f}}, sq[2] = {{0.f, 0.f}, {0.f, 0.f}};
#pragma unroll
    for (int c = 0; c < 8; ++c) {
      sts128(lds + xs + c * 4096, xr[c][0]);
      sts128(lds + xs + c * 4096 + 128, xr[c][1]);
      sts128(lds + wsb + (c & 3) * 16384, wr[c][0]);
      sts128(lds + wsb + (c & 3) * 16384 + 1024, wr[c][1]);
      sts128(lds + wsb + (c & 3) * 16384 + 8192, wr[c][2]);
      sts128(lds + wsb + (c & 3) * 16384 + 8192 + 1024, wr[c][3]);
      BAR();
      if (c == 0) STAMP(1);
      const int cc = (c + rot) & 7;
      wq[c][0] = gld(rq, wv, cc * 196608);
      wq[c][1] = gld(rq, wv + 24576, cc * 196608);
      SBAR();
#pragma unroll
      for (int i = 0; i < 2; ++i)
#pragma unroll
        for (int d = 0; d < 4; ++d) {
          const f32x2 v = {bflo(xr[c][i][d]), bfhi(xr[c][i][d])};
          sm[i] += v; sq[i] += v * v;
        }
      SBAR();
    }
    STAMP(2);
#pragma unroll
    for (int i = 0; i < 2; ++i) {
      float s = sm[i].x + sm[i].y, qq = sq[i].x + sq[i].y;
#pragma unroll
      for (int mk = 1; mk < 8; mk <<= 1) { s += __shfl_xor(s, mk); qq += __shfl_xor(qq, mk); }
      const float mu = s * (1.f / DM), var = fmaxf(qq * (1.f / DM) - mu * mu, 0.f), r = rsqrtf(var + EPS);
      if (c8 == 0) {
        const int row = 16 * hw + 8 * i + l8;
        *(LAS f32x2*)(lds + L_ST1 + row * 8) = f32x2{mu, r};
        *(LAS float*)(lds + L_R1 + row * 4) = r;
      }
    }
    BAR();
#pragma unroll
    for (int f = 0; f < 4; ++f) {
      sts128(lds + wsb + f * 16384, wq[2 * f][0]);
      sts128(lds + wsb + f * 16384 + 1024, wq[2 * f][1]);
      sts128(lds + wsb + f * 16384 + 8192, wq[2 * f + 1][0]);
      sts128(lds + wsb + f * 16384 + 8192 + 1024, wq[2 * f + 1][1]);
      BAR();
      if (f == 2 && W == 4 && lane == 0) __hip_atomic_store(flags + (0 * 256 + L) * 16, tag, RLX_AGENT);
    }
    BAR();
    STAMP(3);
    Att<1> att;
    att.init(lane, hw & 1, hw >> 1, tb, lds128(lds + L_Q1 + hw * 1024 + lane * 16));
    att.own(lds);
    const int ptb = tb - 4 + hw;
    if (ptb >= 0) {
      wait_flags(flags + (0 * 256 + ptb * 8 + n) * 16, tag);
      STAMP(4);
      v4u kv[16];
#pragma unroll
      for (int i = 0; i < 16; ++i) kv[i] = gld1(rws, lane * 16 + (i & 7) * 1024, WS_KV + (2 * ptb + (i >> 3)) * 65536 + n * 8192);
      SBAR();
#pragma unroll
      for (int i = 0; i < 16; ++i) sts128(lds + L_KV + hw * 16384 + i * 1024 + lane * 16, kv[i]);
    }
    const rsrc_t r1 = mkrsrc(w1, DM * FF * 2);
    v4u w1r[8][3];
    int w1s[3];
#pragma unroll
    for (int e = 0; e < 3; ++e) {
      const int qi = e * 64 + lane, row = qi / 12, ch = qi - row * 12, ct = ch >> 2;
      const int go = ct < nt ? (16 * hw + row) * (FF * 2) + f0 * 64 + ch * 16 : 0x40000000;
      w1s[e] = L_W1 + hw * 3072 + ct * 1024 + (row ^ (ct & 1)) * 64 + (ch & 3) * 16;
#pragma unroll
      for (int i = 0; i < 8; ++i) w1r[i][e] = gld(r1, go, i * 64 * (FF * 2));
    }
    BAR();
    STAMP(5);
    const v4u of = att.rest(lds);
    gst1(rws, of, lane * 16, WS_O + (2 * tb + (hw & 1)) * 32768 + (4 * n + 2 * (hw >> 1) + 1) * 1024);
    STAMP(6);
    DRAIN();
    BAR();
    if (W == 4 && lane == 0) __hip_atomic_store(flags + (1 * 256 + L) * 16, tag, RLX_AGENT);
#pragma unroll
    for (int i = 2; i < 8; ++i)
#pragma unroll
      for (int e = 0; e < 3; ++e) sts128(lds + w1s[e] + i * 12288, w1r[i][e]);
    const rsrc_t r2 = mkrsrc(w2, FF * DM * 2);
    v4u w2r[31];
    const int w2v = ((hw & 1) * 8 + l8) * 1024 + n * 128 + c8 * 16;
#pragma unroll
    for (int u = 12; u < 43; ++u) w2r[u - 12] = gld(r2, w2v, (43 * (hw >> 1) + rot43(u, rotE)) * 16384);
    BAR();
    BAR();
#pragma unroll
    for (int i = 0; i < 2; ++i)
#pragma unroll
      for (int e = 0; e < 3; ++e) sts128(lds + w1s[e] + i * 12288, w1r[i][e]);
    BAR();
    if (W == 4 && lane == 0) __hip_atomic_store(flags + (2 * 256 + L) * 16, tag, RLX_AGENT);
    if (hw < 3) {
      const unsigned one = ((lane >> 4) & 1) == ((lane >> 2) & 1) ? 0x3f803f80u : 0u;
      const v4u sel = {one, one, one, one};
      const lptr wp = lds + L_W1 + hw * 1024 + (((4 * h + q4) ^ (hw & 1)) * 64) + trc;
      f32x4 cs = {0.f, 0.f, 0.f, 0.f};
#pragma unroll 8
      for (int ks = 0; ks < 32; ++ks) cs = mfma16(sel, trfrag(wp + ks * 3072, 0, 512), cs);
      if (h == 0) *(LAS float*)(lds + L_CS2 + (hw * 32 + t) * 4) = cs[0];
    }
    BAR();
    {
      float s = 0.f, qq = 0.f;
      v2u pr[8];
#pragma unroll
      for (int i = 0; i < 8; ++i) pr[i] = __builtin_amdgcn_raw_buffer_load_b64(rws, (8 * h + i) * 256 + t * 8, WS_HST + (4 * cl + hw) * 4096, SC1);
#pragma unroll
      for (int i = 0; i < 8; ++i) { s += __uint_as_float(pr[i].x); qq += __uint_as_float(pr[i].y); }
      s = xhalf_sum(s); qq = xhalf_sum(qq);
      const float mu = s * (1.f / DM), var = fmaxf(qq * (1.f / DM) - mu * mu, 0.f);
      if (h == 0) *(LAS f32x2*)(lds + L_ST2 + (hw * 32 + t) * 8) = f32x2{mu, var + EPS};
    }
    BAR();
    const int w2s = (hw & 1) * 1024 + wst;
#pragma unroll
    for (int u = 12; u < 36; ++u) sts128(lds + w2s + w2_lds(hw >> 1, u), w2r[u - 12]);
    BAR();
    if (W == 4 && lane == 0) __hip_atomic_store(flags + (3 * 256 + L) * 16, tag, RLX_AGENT);
#pragma unroll
    for (int u = 36; u < 43; ++u) sts128(lds + w2s + w2_lds(hw >> 1, u), w2r[u - 12]);
    BAR();
    if (L == 0 && W == 7) {
#pragma unroll
      for (int i = 0; i < 4; ++i) wait_flags(flags + (i * 64 + lane) * 16, tag);
      if (lane == 0) __hip_atomic_store((GAS u64*)ws, tag, RLX_AGENT);
    }
    BAR();
  } else {
    const int m = W & 1, p = W >> 1;
    const u64 tag = __hip_atomic_load((GAS u64*)ws, RLX_AGENT) * 6364136223846793005ull + 1442695040888963407ull;
    const unsigned one = ((lane >> 4) & 1) == ((lane >> 2) & 1) ? 0x3f803f80u : 0u;
    const v4u sel = {one, one, one, one};
    f32x16 zero;
#pragma unroll
    for (int r = 0; r < 16; ++r) zero[r] = 0.f;
    lptr xa[4];
#pragma unroll
    for (int i = 0; i < 4; ++i) xa[i] = lds + L_X + m * 32768 + i * 1024 + ((lane ^ (2 * i + h)) * 16);
    const lptr wa = lds + L_RING + (8 * h + q4) * 128 + ((p ^ (q4 >> 1)) * 64) + trc;
    f32x16 ak = zero, av = zero;
    f32x4 ck = {0.f, 0.f, 0.f, 0.f}, cv = ck;
#pragma unroll
    for (int c = 0; c < 8; ++c) {
      BAR();
#pragma unroll
      for (int ks = 0; ks < 4; ++ks) {
        const v4u xf = lds128(xa[ks] + c * 4096);
        const v4u kf = trfrag(wa + (c & 3) * 16384 + ks * 2048, 0, 512);
        const v4u vf = trfrag(wa + (c & 3) * 16384 + ks * 2048 + 8192, 0, 512);
        ak = mfma32(kf, xf, ak);
        av = mfma32(xf, vf, av);
        ck = mfma16(sel, kf, ck);
        cv = mfma16(sel, vf, cv);
      }
    }
    STAMP(1);
    BAR();
    const f32x2 st = *(LAS f32x2*)(lds + L_ST1 + (m * 32 + t) * 8);
    v4u mf;
    {
      float hi, lo; split2(-st.x, hi, lo);
      mf = v4u{h ? 0u : pk2(hi, hi), h ? 0u : pk2(lo, 0.f), 0u, 0u};
    }
    auto cfrag = [&](float c) { float hi, lo; split2(c, hi, lo); return v4u{pk2(hi, lo), pk2(hi, 0.f), 0u, 0u}; };
    ak = mfma32(cfrag(ck[0]), mf, ak);
    av = mfma32(mf, cfrag(cv[0]), av);
#pragma unroll
    for (int g = 0; g < 4; ++g) {
      const f32x4 rr = *(LAS f32x4*)(lds + L_R1 + (m * 32 + 8 * g + 4 * h) * 4);
#pragma unroll
      for (int e = 0; e < 4; ++e) { ak[4 * g + e] *= st.y; av[4 * g + e] *= rr[e]; }
    }
    {
      const v4u f[4] = {pack8<0>(ak), pack8<8>(ak), pack8<0>(av), pack8<8>(av)};
#pragma unroll
      for (int i = 0; i < 4; ++i) {
        gst1(rws, f[i], lane * 16 + i * 1024, WS_KV + (2 * tb + m) * 65536 + (2 * n + p) * 4096);
        sts128(lds + L_KV + (8 + m) * 8192 + p * 4096 + i * 1024 + lane * 16, f[i]);
      }
    }
    STAMP(2);
    f32x16 aq = zero;
    f32x4 cq = {0.f, 0.f, 0.f, 0.f};
#pragma unroll
    for (int f = 0; f < 4; ++f) {
      if (f == 2) DRAIN();
      BAR();
#pragma unroll
      for (int ks = 0; ks < 8; ++ks) {
        const v4u xf = lds128(xa[ks & 3] + (2 * f + (ks >> 2)) * 4096);
        const v4u qf = trfrag(wa + f * 16384 + ks * 2048, 0, 512);
        aq = mfma32(qf, xf, aq);
        cq = mfma16(sel, qf, cq);
      }
    }
    aq = mfma32(cfrag(cq[0]), mf, aq);
    v2u xres[4];
    {
      const int c = (n - rot) & 7;
#pragma unroll
      for (int g = 0; g < 4; ++g) {
        const int ks = 2 * p + (g >> 1), hh = g & 1;
        xres[g] = *(LAS v2u*)(lds + L_X + m * 32768 + (4 * c + ks) * 1024 + (((32 * hh + t) ^ (2 * ks + hh)) * 16) + 8 * h);
      }
    }
    {
      const float qs = st.y * QSCALE;
#pragma unroll
      for (int r = 0; r < 16; ++r) aq[r] *= qs;
    }
    sts128(lds + L_Q1 + W * 1024 + lane * 16, pack8<8>(aq));
    Att<0> att;
    att.init(lane, m, p, tb, pack8<0>(aq));
    BAR();
    STAMP(3);
    const rsrc_t ro = mkrsrc(wo, DM * DM * 2), r2 = mkrsrc(w2, FF * DM * 2);
    v4u wor[16], w2r[12];
    const int wov = l8 * 1024 + n * 128 + c8 * 16;
#pragma unroll
    for (int i = 0; i < 16; ++i) wor[i] = gld(ro, wov, (W * 128 + i * 8) * 1024);
    SBAR();
    att.own(lds);
    BAR();
    STAMP(5);
#pragma unroll
    for (int u = 0; u < 12; ++u) w2r[u] = gld(r2, wov + m * 8192, (43 * p + rot43(u, rotE)) * 16384);
    SBAR();
    const v4u of = att.rest(lds);
    gst1(rws, of, lane * 16, WS_O + (2 * tb + m) * 32768 + (4 * n + 2 * p) * 1024);
    STAMP(6);
#pragma unroll
    for (int i = 0; i < 8; ++i) sts128(lds + L_WO + W * 16384 + i * 1024 + wst, wor[i]);
    DRAIN();
    BAR();
#pragma unroll
    for (int i = 8; i < 16; ++i) sts128(lds + L_WO + W * 16384 + i * 1024 + wst, wor[i]);
    f32x16 a0, a1 = zero;
#pragma unroll
    for (int g = 0; g < 4; ++g) { a0[4 * g] = bflo(xres[g].x); a0[4 * g + 1] = bfhi(xres[g].x); a0[4 * g + 2] = bflo(xres[g].y); a0[4 * g + 3] = bfhi(xres[g].y); }
    const int trp = (4 * h + q4) * 128 + trc;
    const int own64 = (p ^ (q4 >> 1)) * 64;
    if (W == 0) wait_flags(flags + (1 * 256 + tb * 8 + (lane & 7)) * 16, tag);
    BAR();
    STAMP(7);
    {
      v4u of_[16];
#pragma unroll
      for (int i = 0; i < 16; ++i) of_[i] = gld1(rws, lane * 16, WS_O + (2 * tb + m) * 32768 + (16 * p + ((i + 2 * n) & 15)) * 1024);
      SBAR();
#pragma unroll
      for (int i = 0; i < 16; ++i) {
        const lptr wp = lds + L_WO + (16 * p + ((i + 2 * n) & 15)) * 2048 + trp;
        a0 = mfma32(trfrag(wp + own64, 0, 1024), of_[i], a0);
        a1 = mfma32(trfrag(wp + (own64 ^ 64), 0, 1024), of_[i], a1);
      }
    }
#pragma unroll
    for (int g = 0; g < 4; ++g) *(LAS f32x4*)(lds + L_XCH + W * 4096 + g * 1024 + lane * 16) = f32x4{a1[4 * g], a1[4 * g + 1], a1[4 * g + 2], a1[4 * g + 3]};
    BAR();
    STAMP(8);
#pragma unroll
    for (int g = 0; g < 4; ++g) {
      const f32x4 v = *(LAS f32x4*)(lds + L_XCH + (W ^ 2) * 4096 + g * 1024 + lane * 16);
#pragma unroll
      for (int e = 0; e < 4; ++e) a0[4 * g + e] += v[e];
    }
    gst1(rws, pack8<0>(a0), lane * 16, WS_H + (2 * tb + m) * 32768 + (4 * n + 2 * p) * 1024);
    gst1(rws, pack8<8>(a0), lane * 16 + 1024, WS_H + (2 * tb + m) * 32768 + (4 * n + 2 * p) * 1024);
    {
      float s = 0.f, qq = 0.f;
#pragma unroll
      for (int r = 0; r < 16; ++r) { s += a0[r]; qq += a0[r] * a0[r]; }
      s = xhalf_sum(s); qq = xhalf_sum(qq);
      if (h == 0)
        __builtin_amdgcn_raw_buffer_store_b64(v2u{__builtin_bit_cast(unsigned, s), __builtin_bit_cast(unsigned, qq)}, rws, t * 8,
                                              WS_HST + (2 * tb + m) * 4096 + (2 * n + p) * 256, SC1);
    }
    STAMP(9);
#pragma unroll
    for (int u = 0; u < 12; ++u) sts128(lds + m * 1024 + wst + w2_lds(p, u), w2r[u]);
    DRAIN();
    BAR();
    {
      const rsrc_t rb = mkrsrc(b2, DM * 2);
#pragma unroll
      for (int g = 0; g < 4; ++g) {
        const v2u bb = __builtin_amdgcn_raw_buffer_load_b64(rb, (8 * g + 4 * h) * 2, (64 * n + 32 * p) * 2, 0);
        a0[4 * g] += bflo(bb.x); a0[4 * g + 1] += bfhi(bb.x); a0[4 * g + 2] += bflo(bb.y); a0[4 * g + 3] += bfhi(bb.y);
      }
    }
    unsigned b1v[3];
    {
      const rsrc_t rb = mkrsrc(b1, FF * 2);
#pragma unroll
      for (int ct = 0; ct < 3; ++ct) b1v[ct] = __builtin_amdgcn_raw_buffer_load_b16(rb, t * 2 + ct * 64, f0 * 64, 0);
    }
    if (W == 0) wait_flags(flags + (2 * 256 + cl * 16 + (lane & 15)) * 16, tag);
    BAR();
    STAMP(10);
    f32x16 d0 = zero, d1 = zero, d2 = zero;
    {
      v4u hf[32];
#pragma unroll
      for (int i = 0; i < 32; ++i) hf[i] = gld1(rws, lane * 16, WS_H + (4 * cl + W) * 32768 + ((i + 2 * j) & 31) * 1024);
      SBAR();
      const int e0 = (4 * h + q4) * 64 + trc;
#pragma unroll
      for (int i = 0; i < 32; ++i) {
        const lptr wp = lds + L_W1 + ((i + 2 * j) & 31) * 3072;
        d0 = mfma32(trfrag(wp + e0, 0, 512), hf[i], d0);
        d1 = mfma32(trfrag(wp + (e0 ^ 64) + 1024, 0, 512), hf[i], d1);
        d2 = mfma32(trfrag(wp + e0 + 2048, 0, 512), hf[i], d2);
      }
    }
    BAR();
    STAMP(11);
    {
      const f32x2 s2 = *(LAS f32x2*)(lds + L_ST2 + (W * 32 + t) * 8);
      const float sd = sqrtf(s2.y), r2 = 1.f / s2.y;
      float mh, ml, sh, sl; split2(-s2.x, mh, ml); split2(sd, sh, sl);
      const v4u bf = {h ? 0u : pk2(mh, mh), h ? 0u : pk2(ml, sh), h ? 0u : pk2(sl, 0.f), 0u};
      auto fin = [&](f32x16& d, int ct) {
        float ch, cl_; split2(*(LAS float*)(lds + L_CS2 + (ct * 32 + t) * 4), ch, cl_);
        const v4u af = {pk2(ch, cl_), (pk2(ch, 0.f) & 0xffffu) | (b1v[ct] << 16), b1v[ct] & 0xffffu, 0u};
        d = mfma32(af, bf, d);
#pragma unroll
        for (int r = 0; r < 16; ++r) { const float v = fmaxf(d[r], 0.f); d[r] = v * v * r2; }
        if (ct < nt) {
          gst1(rws, pack8<0>(d), lane * 16, WS_H1 + (4 * cl + W) * 88064 + (f0 + ct) * 2048);
          gst1(rws, pack8<8>(d), lane * 16 + 1024, WS_H1 + (4 * cl + W) * 88064 + (f0 + ct) * 2048);
        }
      };
      fin(d0, 0); fin(d1, 1); fin(d2, 2);
    }
    STAMP(12);
    v4u wh[7][2];
#pragma unroll
    for (int u = 0; u < 7; ++u) {
      wh[u][0] = trfrag(lds + w2_lds(p, u) + trp + own64, 0, 1024);
      wh[u][1] = trfrag(lds + w2_lds(p, u) + trp + (own64 ^ 64), 0, 1024);
    }
    DRAIN();
    BAR();
    if (W == 0) wait_flags(flags + (3 * 256 + cl * 16 + (lane & 15)) * 16, tag);
    BAR();
    STAMP(13);
    a1 = zero;
    {
      constexpr int WIN = 24;
      v4u gf[WIN];
      const int gbase = WS_H1 + (2 * tb + m) * 88064 + 43 * p * 1024;
#pragma unroll
      for (int u = 0; u < WIN; ++u) gf[u] = gld1(rws, lane * 16, gbase + rot43(u, rotE) * 1024);
      SBAR();
#pragma unroll
      for (int u = 0; u < 43; ++u) {
        const v4u g = gf[u % WIN];
        if (u < 7) {
          a0 = mfma32(wh[u][0], g, a0);
          a1 = mfma32(wh[u][1], g, a1);
        } else {
          const lptr wp = lds + w2_lds(p, u) + trp;
          a0 = mfma32(trfrag(wp + own64, 0, 1024), g, a0);
          a1 = mfma32(trfrag(wp + (own64 ^ 64), 0, 1024), g, a1);
        }
        if (u + WIN < 43) { gf[u % WIN] = gld1(rws, lane * 16, gbase + rot43(u + WIN, rotE) * 1024); SBAR(); }
      }
    }
#pragma unroll
    for (int g = 0; g < 4; ++g) *(LAS f32x4*)(lds + L_XCH + W * 4096 + g * 1024 + lane * 16) = f32x4{a1[4 * g], a1[4 * g + 1], a1[4 * g + 2], a1[4 * g + 3]};
    BAR();
    STAMP(14);
#pragma unroll
    for (int g = 0; g < 4; ++g) {
      const f32x4 v = *(LAS f32x4*)(lds + L_XCH + (W ^ 2) * 4096 + g * 1024 + lane * 16);
#pragma unroll
      for (int e = 0; e < 4; ++e) a0[4 * g + e] += v[e];
    }
    const rsrc_t rout = mkrsrc(out, SEQ * DM * 2);
#pragma unroll
    for (int k = 0; k < 2; ++k) {
      const v4u o = k ? pack8<8>(a0) : pack8<0>(a0);
      auto s0 = __builtin_amdgcn_permlane32_swap(o.x, o.z, false, false);
      auto s1 = __builtin_amdgcn_permlane32_swap(o.y, o.w, false, false);
      __builtin_amdgcn_raw_buffer_store_b128(v4u{s0[0], s1[0], s0[1], s1[1]}, rout, t * 1024 + h * 16 + k * 32,
                                             (tb * 64 + m * 32) * 1024 + n * 128 + p * 64, 0);
    }
    STAMP(15);
  }
  STAMP_OUT();
}
}

extern "C" void kernel_launch(void* const* d_in, const int* in_sizes, int n_in, void* d_out, int out_size, void* d_ws,
                              size_t ws_size, hipStream_t stream) {
  using namespace fb;
  (void)hipFuncSetAttribute((const void*)fused_block, hipFuncAttributeMaxDynamicSharedMemorySize, LDS_BYTES);
  fused_block<<<256, 512, LDS_BYTES, stream>>>((const u16*)d_in[0], (const u16*)d_in[1], (const u16*)d_in[2], (const u16*)d_in[4],
                                               (const u16*)d_in[5], (const u16*)d_in[6], (const u16*)d_in[7], (u16*)d_out, (char*)d_ws);
}
```

```cpp
#include <hip/hip_runtime.h>
#include <hip/hip_bf16.h>

namespace fb {

__global__ void __launch_bounds__(512, 2) __attribute__((aligned(4096)))
fused_block(const void* x, const void* wqkv, const void* wo, const void* w1, const void* b1, const void* w2, const void* b2,
            void* out, void* ws) {
  __shared__ __attribute__((aligned(16))) char smem[163840];
  asm volatile(
      ".Lkbeg_%=:\n"
      "s_load_dwordx2 s[4:5], %0, 0x0\n"
      "s_load_dwordx2 s[8:9], %0, 0x8\n"
      "s_load_dwordx2 s[24:25], %0, 0x40\n"
      "s_load_dwordx8 s[80:87], %0, 0x10\n"
      "s_load_dwordx4 s[88:91], %0, 0x30\n"
      "v_lshrrev_b32 v7, 3, v0\n"
      "v_and_b32 v1, 63, v0\n"
      "v_lshrrev_b32 v0, 6, v0\n"
      "s_lshr_b32 s55, %1, 3\n"
      "s_getpc_b64 s[52:53]\n"
      ".Lpc_%=:\n"
      "s_and_b32 s54, s55, 7\n"
      "s_mul_i32 s54, s54, 40\n"
      "v_min_u32 v9, 4, v1\n"
      "v_mad_u32_u24 v9, v0, 5, v9\n"
      "v_add_lshl_u32 v9, v9, s54, 7\n"
      "v_min_u32 v9, .Lkend_%=-.Lpc_%=-4, v9\n"
      "global_load_dword v9, v9, s[52:53]\n"
      "v_readfirstlane_b32 s38, v0\n"
      "s_and_b32 s35, %1, 6\n"
      "s_lshl_b32 s39, s35, 2\n"
      "s_lshr_b32 s56, %1, 5\n"
      "s_add_u32 s39, s39, s56\n"
      "s_and_b32 s40, %1, 1\n"
      "s_lshl_b32 s40, s40, 2\n"
      "s_and_b32 s57, s55, 3\n"
      "s_add_u32 s40, s40, s57\n"
      "v_and_b32 v4, 7, v1\n"
      "s_lshl_b32 s57, s39, 16\n"
      "v_lshl_add_u32 v7, v7, 10, s57\n"
      "v_lshl_add_u32 v7, v4, 4, v7\n"
      "v_min_u32 v8, 5, v1\n"
      "v_mad_u32_u24 v8, v0, 6, v8\n"
      "v_lshlrev_b32 v8, 7, v8\n"
      "s_mul_i32 s56, %1, 6144\n"
      "s_lshl_b32 s51, s40, 7\n"
      "s_mov_b32 s6, 2097152\n"
      "s_mov_b32 s7, 0x20000\n"
      "s_mov_b32 s10, 1572864\n"
      "s_mov_b32 s11, 0x20000\n"
      "s_mov_b32 s26, 33570816\n"
      "s_mov_b32 s27, 0x20000\n"
      "s_lshl_b32 s41, s39, 3\n"
      "s_add_u32 s41, s41, s40\n"
      "s_lshr_b32 s43, s39, 1\n"
      "s_and_b32 s44, s41, 15\n"
      "s_lshl_b32 s57, s43, 21\n"
      "s_lshl_b32 s59, s44, 7\n"
      "s_add_u32 s57, s57, s59\n"
      "s_add_u32 s57, s57, 1073152\n"
      "v_min_u32 v10, 2, v1\n"
      "v_cmp_eq_u32 vcc, 0, v10\n"
      "v_lshl_add_u32 v10, v10, 13, s57\n"
      "v_mov_b32 v11, 33554432\n"
      "v_cndmask_b32 v10, v10, v11, vcc\n"
      "v_and_b32 v12, 15, v1\n"
      "v_bfe_u32 v14, v1, 4, 1\n"
      "v_lshlrev_b32 v14, 13, v14\n"
      "s_lshl_b32 s58, s44, 3\n"
      "s_add_u32 s58, s58, s38\n"
      "s_cmp_eq_u32 s58, 4\n"
      "s_cselect_b32 s58, 2064384, 1073741824\n"
      "v_lshl_add_u32 v12, v12, 2, v14\n"
      "v_add_u32 v12, s58, v12\n"
      "v_mov_b32 v14, 1\n"
      "s_lshl_b32 s58, s43, 21\n"
      "s_waitcnt lgkmcnt(0)\n"
      "s_and_b32 s5, s5, 0xffff\n"
      "buffer_load_dwordx4 v[16:19], v7, s[4:7], s51 offen\n"
      "s_and_b32 s9, s9, 0xffff\n"
      "buffer_load_dword v8, v8, s[8:11], s56 offen\n"
      "s_add_u32 s24, s24, 2097151\n"
      "s_addc_u32 s25, s25, 0\n"
      "s_and_b32 s24, s24, 0xffe00000\n"
      "s_and_b32 s25, s25, 0xffff\n"
      "buffer_load_dwordx2 v[10:11], v10, s[24:27], 0 offen sc1\n"
      "buffer_store_dword v14, v12, s[24:27], s58 offen sc1\n"
      "s_barrier\n"
      "s_cmp_ge_u32 s38, 4\n"
      "s_cbranch_scc1 .Lhelper_%=\n"
      "s_add_u32 s51, s51, 128\n"
      "s_and_b32 s51, s51, 0x3ff\n"
      "buffer_load_dwordx4 v[20:23], v7, s[4:7], s51 offen\n"
      "s_add_u32 s51, s51, 128\n"
      "s_and_b32 s51, s51, 0x3ff\n"
      "buffer_load_dwordx4 v[24:27], v7, s[4:7], s51 offen\n"
      "s_add_u32 s51, s51, 128\n"
      "s_and_b32 s51, s51, 0x3ff\n"
      "buffer_load_dwordx4 v[28:31], v7, s[4:7], s51 offen\n"
      "s_add_u32 s51, s51, 128\n"
      "s_and_b32 s51, s51, 0x3ff\n"
      "buffer_load_dwordx4 v[32:35], v7, s[4:7], s51 offen\n"
      "s_add_u32 s51, s51, 128\n"
      "s_and_b32 s51, s51, 0x3ff\n"
      "buffer_load_dwordx4 v[36:39], v7, s[4:7], s51 offen\n"
      "s_branch .Lcompute_%=\n"
      ".Lret_%=:\n"
      "s_setpc_b64 s[58:59]\n"
      ".Lhelper_%=:\n"
      "s_lshl_b32 s57, s43, 21\n"
      "s_add_u32 s57, s57, 2064384\n"
      "s_add_u32 s90, s24, s57\n"
      "s_addc_u32 s91, s25, 0\n"
      "s_load_dword s33, s[90:91], 0x1000\n"
      "v_lshrrev_b32 v3, 3, v1\n"
      "s_and_b32 s48, s38, 3\n"
      "s_lshr_b32 s52, s8, 7\n"
      "s_add_u32 s52, s52, s40\n"
      "s_and_b32 s52, s52, 7\n"
      "s_and_b32 s53, s39, 7\n"
      "s_mul_i32 s53, s53, 4\n"
      "s_lshr_b32 s53, s53, 2\n"
      "s_cmp_eq_u32 s52, 3\n"
      "s_cselect_b32 s53, s53, 0\n"
      "s_add_u32 s42, s53, s35\n"
      "s_sub_u32 s52, s42, 8\n"
      "s_cselect_b32 s42, s42, s52\n"
      "s_lshl_b32 s56, s48, 4\n"
      "v_add_u32 v48, s56, v3\n"
      "v_lshlrev_b32 v13, 1, v3\n"
      "v_and_b32 v13, 4, v13\n"
      "v_xor_b32 v13, v13, v4\n"
      "s_lshl_b32 s57, s40, 7\n"
      "v_lshl_add_u32 v49, v13, 4, s57\n"
      "s_movk_i32 s57, 0xc00\n"
      "v_mad_u32_u24 v13, v48, s57, v49\n"
      "v_add_u32 v48, 0x6000, v13\n"
      "s_add_u32 s64, s42, 0\n"
      "s_and_b32 s64, s64, 7\n"
      "s_mul_i32 s64, s64, 0x30000\n"
      "s_add_u32 s65, s42, 1\n"
      "s_and_b32 s65, s65, 7\n"
      "s_mul_i32 s65, s65, 0x30000\n"
      "s_add_u32 s66, s42, 2\n"
      "s_and_b32 s66, s66, 7\n"
      "s_mul_i32 s66, s66, 0x30000\n"
      "s_add_u32 s67, s42, 3\n"
      "s_and_b32 s67, s67, 7\n"
      "s_mul_i32 s67, s67, 0x30000\n"
      "s_add_u32 s68, s42, 4\n"
      "s_and_b32 s68, s68, 7\n"
      "s_mul_i32 s68, s68, 0x30000\n"
      "s_add_u32 s69, s42, 5\n"
      "s_and_b32 s69, s69, 7\n"
      "s_mul_i32 s69, s69, 0x30000\n"
      "s_add_u32 s70, s42, 6\n"
      "s_and_b32 s70, s70, 7\n"
      "s_mul_i32 s70, s70, 0x30000\n"
      "s_add_u32 s71, s42, 7\n"
      "s_and_b32 s71, s71, 7\n"
      "s_mul_i32 s71, s71, 0x30000\n"
      "s_lshl_b32 s77, s48, 12\n"
      "s_add_u32 s77, s77, 65536\n"
      "v_and_b32 v50, 1, v4\n"
      "v_lshlrev_b32 v50, 5, v50\n"
      "s_lshl_b32 s52, s48, 3\n"
      "v_add3_u32 v50, v50, s52, v3\n"
      "v_xor_b32 v50, v50, v4\n"
      "v_lshlrev_b32 v50, 4, v50\n"
      "v_lshrrev_b32 v51, 1, v4\n"
      "v_lshl_add_u32 v50, v51, 10, v50\n"
      "v_add_u32 v51, 159744, v50\n"
      "s_sub_u32 s52, s40, s42\n"
      "s_lshl_b32 s52, s52, 12\n"
      "s_and_b32 s52, s52, 0x7fff\n"
      "v_add_u32 v52, s52, v50\n"
      "s_add_u32 s52, s52, 0x1000\n"
      "s_and_b32 s52, s52, 0x7fff\n"
      "v_add_u32 v53, s52, v50\n"
      "s_add_u32 s52, s52, 0x1000\n"
      "s_and_b32 s52, s52, 0x7fff\n"
      "v_add_u32 v54, s52, v50\n"
      "s_add_u32 s52, s52, 0x1000\n"
      "s_and_b32 s52, s52, 0x7fff\n"
      "v_add_u32 v55, s52, v50\n"
      "s_add_u32 s52, s52, 0x1000\n"
      "s_and_b32 s52, s52, 0x7fff\n"
      "v_add_u32 v56, s52, v50\n"
      "s_add_u32 s52, s52, 0x1000\n"
      "s_and_b32 s52, s52, 0x7fff\n"
      "v_add_u32 v57, s52, v50\n"
      "s_add_u32 s52, s52, 0x1000\n"
      "s_and_b32 s52, s52, 0x7fff\n"
      "v_add_u32 v58, s52, v50\n"
      "s_add_u32 s52, s52, 0x1000\n"
      "s_and_b32 s52, s52, 0x7fff\n"
      "v_add_u32 v50, s52, v50\n"
      "v_lshlrev_b32 v2, 4, v1\n"
      "v_and_b32 v5, 31, v1\n"
      "v_lshrrev_b32 v6, 5, v1\n"
      "s_and_b32 s49, s38, 1\n"
      "s_lshr_b32 s50, s48, 1\n"
      "s_and_b32 s75, s39, 1\n"
      "s_lshl_b32 s75, s75, 1\n"
      "s_add_u32 s75, s75, s49\n"
      "s_mov_b32 s12, s80\n"
      "s_and_b32 s13, s81, 0xffff\n"
      "s_mov_b32 s14, 524288\n"
      "s_mov_b32 s15, 0x20000\n"
      "s_mov_b32 s16, s82\n"
      "s_and_b32 s17, s83, 0xffff\n"
      "s_mov_b32 s18, 1409024\n"
      "s_mov_b32 s19, 0x20000\n"
      "s_mov_b32 s20, s86\n"
      "s_and_b32 s21, s87, 0xffff\n"
      "s_mov_b32 s22, 1409024\n"
      "s_mov_b32 s23, 0x20000\n"
      "s_mov_b64 s[92:93], s[84:85]\n"
      "s_mul_i32 s52, s44, 3\n"
      "s_lshl_b32 s53, s44, 1\n"
      "s_add_u32 s53, s53, 11\n"
      "s_cmp_lt_u32 s44, 11\n"
      "s_cselect_b32 s45, s52, s53\n"
      "s_cselect_b32 s46, 3, 2\n"
      "s_lshr_b32 s52, s12, 7\n"
      "s_add_u32 s52, s52, s40\n"
      "s_and_b32 s52, s52, 7\n"
      "s_and_b32 s53, s39, 7\n"
      "s_mul_i32 s53, s53, 4\n"
      "s_lshr_b32 s53, s53, 2\n"
      "s_cmp_eq_u32 s52, 3\n"
      "s_cselect_b32 s53, s53, 0\n"
      "s_add_u32 s78, s53, s35\n"
      "s_sub_u32 s52, s78, 8\n"
      "s_cselect_b32 s78, s78, s52\n"
      "s_lshr_b32 s52, s20, 7\n"
      "s_add_u32 s52, s52, s40\n"
      "s_and_b32 s52, s52, 7\n"
      "s_and_b32 s53, s39, 7\n"
      "s_mul_i32 s53, s53, 4\n"
      "s_lshr_b32 s53, s53, 2\n"
      "s_cmp_eq_u32 s52, 3\n"
      "s_cselect_b32 s53, s53, 0\n"
      "s_add_u32 s47, s53, s35\n"
      "s_sub_u32 s52, s47, 8\n"
      "s_cselect_b32 s47, s47, s52\n"
      "s_cmp_lt_u32 s48, 2\n"
      "s_cselect_b32 s52, 0x3f803f80, 0\n"
      "v_mov_b32 v70, s52\n"
      "v_mov_b32 v71, s52\n"
      "v_mov_b32 v72, s52\n"
      "v_mov_b32 v73, s52\n"
      "s_lshl_b32 s52, s48, 10\n"
      "s_add_u32 s52, s52, 148480\n"
      "v_add_u32 v74, s52, v2\n"
      "ds_write_b128 v74, v[70:73] offset:0\n"
      "v_add_u32 v70, 0, v1\n"
      "v_mul_u32_u24 v71, 0x1556, v70\n"
      "v_lshrrev_b32 v71, 16, v71\n"
      "v_mul_u32_u24 v72, 12, v71\n"
      "v_sub_u32 v70, v70, v72\n"
      "v_mul_u32_u24 v59, 2752, v71\n"
      "v_lshrrev_b32 v72, 2, v70\n"
      "v_cmp_gt_u32 vcc, s46, v72\n"
      "v_lshl_add_u32 v59, v70, 4, v59\n"
      "v_mov_b32 v70, 1073741824\n"
      "v_cndmask_b32 v59, v70, v59, vcc\n"
      "v_add_u32 v70, 64, v1\n"
      "v_mul_u32_u24 v71, 0x1556, v70\n"
      "v_lshrrev_b32 v71, 16, v71\n"
      "v_mul_u32_u24 v72, 12, v71\n"
      "v_sub_u32 v70, v70, v72\n"
      "v_mul_u32_u24 v60, 2752, v71\n"
      "v_lshrrev_b32 v72, 2, v70\n"
      "v_cmp_gt_u32 vcc, s46, v72\n"
      "v_lshl_add_u32 v60, v70, 4, v60\n"
      "v_mov_b32 v70, 1073741824\n"
      "v_cndmask_b32 v60, v70, v60, vcc\n"
      "v_add_u32 v70, 128, v1\n"
      "v_mul_u32_u24 v71, 0x1556, v70\n"
      "v_lshrrev_b32 v71, 16, v71\n"
      "v_mul_u32_u24 v72, 12, v71\n"
      "v_sub_u32 v70, v70, v72\n"
      "v_mul_u32_u24 v61, 2752, v71\n"
      "v_lshrrev_b32 v72, 2, v70\n"
      "v_cmp_gt_u32 vcc, s46, v72\n"
      "v_lshl_add_u32 v61, v70, 4, v61\n"
      "v_mov_b32 v70, 1073741824\n"
      "v_cndmask_b32 v61, v70, v61, vcc\n"
      "s_mul_i32 s73, s48, 3072\n"
      "s_add_u32 s73, s73, 49152\n"
      "s_mul_i32 s74, s48, 44032\n"
      "s_lshl_b32 s52, s45, 6\n"
      "s_add_u32 s74, s74, s52\n"
      "s_lshl_b32 s52, s49, 3\n"
      "v_add_u32 v62, s52, v3\n"
      "v_lshl_add_u32 v62, v62, 10, v49\n"
      "s_mul_i32 s52, s50, 24576\n"
      "s_lshl_b32 s53, s49, 10\n"
      "s_add_u32 s62, s52, s53\n"
      "v_mov_b32 v64, 0\n"
      "v_mov_b32 v65, 1073741824\n"
      "s_min_u32 s55, s48, 2\n"
      "s_add_u32 s52, s45, s55\n"
      "s_min_u32 s52, s52, 42\n"
      "s_lshl_b32 s52, s52, 6\n"
      "v_lshl_add_u32 v68, v5, 1, s52\n"
      "s_mul_i32 s52, s48, 4\n"
      "s_min_u32 s52, s52, 12\n"
      "s_mul_i32 s53, s41, 16\n"
      "s_add_u32 s52, s52, s53\n"
      "s_lshl_b32 s52, s52, 7\n"
      "s_add_u32 s28, s80, s52\n"
      "s_addc_u32 s29, s81, 0\n"
      "s_mul_i32 s52, s48, 11\n"
      "s_min_u32 s52, s52, 32\n"
      "s_mul_i32 s53, s41, 43\n"
      "s_add_u32 s52, s52, s53\n"
      "s_lshl_b32 s52, s52, 7\n"
      "s_add_u32 s30, s82, s52\n"
      "s_addc_u32 s31, s83, 0\n"
      "s_mul_i32 s52, s48, 11\n"
      "s_min_u32 s52, s52, 32\n"
      "s_mul_i32 s53, s41, 43\n"
      "s_add_u32 s52, s52, s53\n"
      "s_lshl_b32 s52, s52, 7\n"
      "s_add_u32 s88, s86, s52\n"
      "s_addc_u32 s89, s87, 0\n"
      "s_add_u32 s51, s51, 128\n"
      "s_and_b32 s51, s51, 0x3ff\n"
      "buffer_load_dwordx4 v[20:23], v7, s[4:7], s51 offen\n"
      "s_add_u32 s51, s51, 128\n"
      "s_and_b32 s51, s51, 0x3ff\n"
      "buffer_load_dwordx4 v[24:27], v7, s[4:7], s51 offen\n"
      "s_add_u32 s51, s51, 128\n"
      "s_and_b32 s51, s51, 0x3ff\n"
      "buffer_load_dwordx4 v[28:31], v7, s[4:7], s51 offen\n"
      "s_add_u32 s51, s51, 128\n"
      "s_and_b32 s51, s51, 0x3ff\n"
      "buffer_load_dwordx4 v[32:35], v7, s[4:7], s51 offen\n"
      "s_add_u32 s51, s51, 128\n"
      "s_and_b32 s51, s51, 0x3ff\n"
      "buffer_load_dwordx4 v[36:39], v7, s[4:7], s51 offen\n"
      "s_add_u32 s51, s51, 128\n"
      "s_and_b32 s51, s51, 0x3ff\n"
      "buffer_load_dwordx4 v[40:43], v7, s[4:7], s51 offen\n"
      "s_add_u32 s51, s51, 128\n"
      "s_and_b32 s51, s51, 0x3ff\n"
      "buffer_load_dwordx4 v[44:47], v7, s[4:7], s51 offen\n"
      "s_add_u32 m0, s77, 31744\n"
      "s_nop 0\n"
      "buffer_load_dwordx4 v13, s[8:11], s64 offen offset:1024 lds\n"
      "buffer_load_dwordx4 v13, s[8:11], s64 offen offset:2048 lds\n"
      "s_add_u32 m0, s77, 33792\n"
      "s_nop 0\n"
      "buffer_load_dwordx4 v48, s[8:11], s64 offen offset:1024 lds\n"
      "buffer_load_dwordx4 v48, s[8:11], s64 offen offset:2048 lds\n"
      "s_waitcnt vmcnt(12)\n"
      "v_readfirstlane_b32 s52, v10\n"
      "v_readfirstlane_b32 s53, v11\n"
      "s_nop 0\n"
      "s_mul_i32 s36, s52, 0x4c957f2d\n"
      "s_mul_hi_u32 s37, s52, 0x4c957f2d\n"
      "s_mul_i32 s52, s52, 0x5851f42d\n"
      "s_mul_i32 s53, s53, 0x4c957f2d\n"
      "s_add_u32 s52, s52, s53\n"
      "s_add_u32 s37, s37, s52\n"
      "s_add_u32 s36, s36, 0xf767814f\n"
      "s_addc_u32 s37, s37, 0x14057b7e\n"
      "s_lshl_b32 s58, s44, 3\n"
      "s_add_u32 s58, s58, s38\n"
      "s_cmp_eq_u32 s58, 4\n"
      "s_cbranch_scc0 .Lnorace_1001_%=\n"
      "s_waitcnt vmcnt(11)\n"
      "s_waitcnt lgkmcnt(0)\n"
      "s_mov_b32 s34, 0\n"
      "s_load_dword s33, s[90:91], 0x0\n"
      "s_load_dword s34, s[90:91], 0x2000\n"
      "v_and_b32 v12, 15, v1\n"
      "v_lshlrev_b32 v12, 3, v12\n"
      "v_mov_b32 v15, s37\n"
      "s_waitcnt lgkmcnt(1)\n"
      "s_nop 11\n"
      "s_and_b32 s52, s34, 1\n"
      "s_xor_b32 s52, s52, s36\n"
      "v_mov_b32 v14, s52\n"
      "s_nop 1\n"
      ".Lnorace_1001_%=:\n"
      "s_lshl_b32 s53, s43, 7\n"
      "s_add_u32 s53, s53, 33558528\n"
      "buffer_store_dwordx2 v[14:15], v12, s[24:27], s53 offen sc1\n"
      "s_setprio 1\n"
      "v_mov_b32 v12, 0x3f803f80\n"
      "ds_write_b128 v51, v[16:19] offset:0\n"
      "v_dot2_f32_bf16 v10, v16, v12, 0\n"
      "v_dot2_f32_bf16 v14, v16, v16, 0\n"
      "v_dot2_f32_bf16 v11, v17, v12, 0\n"
      "v_dot2_f32_bf16 v15, v17, v17, 0\n"
      "v_dot2_f32_bf16 v10, v18, v12, v10\n"
      "v_dot2_f32_bf16 v14, v18, v18, v14\n"
      "v_dot2_f32_bf16 v11, v19, v12, v11\n"
      "v_dot2_f32_bf16 v15, v19, v19, v15\n"
      "s_waitcnt vmcnt(11)\n"
      "v_dot2_f32_bf16 v10, v20, v12, v10\n"
      "v_dot2_f32_bf16 v14, v20, v20, v14\n"
      "v_dot2_f32_bf16 v11, v21, v12, v11\n"
      "v_dot2_f32_bf16 v15, v21, v21, v15\n"
      "v_dot2_f32_bf16 v10, v22, v12, v10\n"
      "v_dot2_f32_bf16 v14, v22, v22, v14\n"
      "v_dot2_f32_bf16 v11, v23, v12, v11\n"
      "v_dot2_f32_bf16 v15, v23, v23, v15\n"
      "s_waitcnt vmcnt(10)\n"
      "v_dot2_f32_bf16 v10, v24, v12, v10\n"
      "v_dot2_f32_bf16 v14, v24, v24, v14\n"
      "v_dot2_f32_bf16 v11, v25, v12, v11\n"
      "v_dot2_f32_bf16 v15, v25, v25, v15\n"
      "v_dot2_f32_bf16 v10, v26, v12, v10\n"
      "v_dot2_f32_bf16 v14, v26, v26, v14\n"
      "v_dot2_f32_bf16 v11, v27, v12, v11\n"
      "v_dot2_f32_bf16 v15, v27, v27, v15\n"
      "s_waitcnt vmcnt(9)\n"
      "v_dot2_f32_bf16 v10, v28, v12, v10\n"
      "v_dot2_f32_bf16 v14, v28, v28, v14\n"
      "v_dot2_f32_bf16 v11, v29, v12, v11\n"
      "v_dot2_f32_bf16 v15, v29, v29, v15\n"
      "v_dot2_f32_bf16 v10, v30, v12, v10\n"
      "v_dot2_f32_bf16 v14, v30, v30, v14\n"
      "v_dot2_f32_bf16 v11, v31, v12, v11\n"
      "v_dot2_f32_bf16 v15, v31, v31, v15\n"
      "s_waitcnt vmcnt(8)\n"
      "v_dot2_f32_bf16 v10, v32, v12, v10\n"
      "v_dot2_f32_bf16 v14, v32, v32, v14\n"
      "v_dot2_f32_bf16 v11, v33, v12, v11\n"
      "v_dot2_f32_bf16 v15, v33, v33, v15\n"
      "v_dot2_f32_bf16 v10, v34, v12, v10\n"
      "v_dot2_f32_bf16 v14, v34, v34, v14\n"
      "v_dot2_f32_bf16 v11, v35, v12, v11\n"
      "v_dot2_f32_bf16 v15, v35, v35, v15\n"
      "s_waitcnt vmcnt(7)\n"
      "v_dot2_f32_bf16 v10, v36, v12, v10\n"
      "v_dot2_f32_bf16 v14, v36, v36, v14\n"
      "v_dot2_f32_bf16 v11, v37, v12, v11\n"
      "v_dot2_f32_bf16 v15, v37, v37, v15\n"
      "v_dot2_f32_bf16 v10, v38, v12, v10\n"
      "v_dot2_f32_bf16 v14, v38, v38, v14\n"
      "v_dot2_f32_bf16 v11, v39, v12, v11\n"
      "v_dot2_f32_bf16 v15, v39, v39, v15\n"
      "s_waitcnt vmcnt(6)\n"
      "v_dot2_f32_bf16 v10, v40, v12, v10\n"
      "v_dot2_f32_bf16 v14, v40, v40, v14\n"
      "v_dot2_f32_bf16 v11, v41, v12, v11\n"
      "v_dot2_f32_bf16 v15, v41, v41, v15\n"
      "v_dot2_f32_bf16 v10, v42, v12, v10\n"
      "v_dot2_f32_bf16 v14, v42, v42, v14\n"
      "v_dot2_f32_bf16 v11, v43, v12, v11\n"
      "v_dot2_f32_bf16 v15, v43, v43, v15\n"
      "s_waitcnt vmcnt(5)\n"
      "v_dot2_f32_bf16 v10, v44, v12, v10\n"
      "v_dot2_f32_bf16 v14, v44, v44, v14\n"
      "v_dot2_f32_bf16 v11, v45, v12, v11\n"
      "v_dot2_f32_bf16 v15, v45, v45, v15\n"
      "v_dot2_f32_bf16 v10, v46, v12, v10\n"
      "v_dot2_f32_bf16 v14, v46, v46, v14\n"
      "v_dot2_f32_bf16 v11, v47, v12, v11\n"
      "v_dot2_f32_bf16 v15, v47, v47, v15\n"
      "v_lshlrev_b32 v70, 16, v16\n"
      "v_and_b32 v71, 0xffff0000, v16\n"
      "v_lshlrev_b32 v72, 16, v17\n"
      "v_and_b32 v73, 0xffff0000, v17\n"
      "v_lshlrev_b32 v74, 16, v18\n"
      "v_and_b32 v75, 0xffff0000, v18\n"
      "v_lshlrev_b32 v76, 16, v19\n"
      "v_and_b32 v77, 0xffff0000, v19\n"
      "v_add_f32 v10, v10, v11\n"
      "v_add_f32 v14, v14, v15\n"
      "s_nop 0\n"
      "v_add_f32_dpp v10, v10, v10 quad_perm:[1,0,3,2] row_mask:0xf bank_mask:0xf\n"
      "v_add_f32_dpp v14, v14, v14 quad_perm:[1,0,3,2] row_mask:0xf bank_mask:0xf\n"
      "s_nop 0\n"
      "v_add_f32_dpp v10, v10, v10 quad_perm:[2,3,0,1] row_mask:0xf bank_mask:0xf\n"
      "v_add_f32_dpp v14, v14, v14 quad_perm:[2,3,0,1] row_mask:0xf bank_mask:0xf\n"
      "s_nop 0\n"
      "v_add_f32_dpp v10, v10, v10 row_half_mirror row_mask:0xf bank_mask:0xf\n"
      "v_add_f32_dpp v14, v14, v14 row_half_mirror row_mask:0xf bank_mask:0xf\n"
      "v_mul_f32 v10, 0xbb000000, v10\n"
      "v_mul_f32 v14, 0x3b000000, v14\n"
      "v_fma_f32 v14, -v10, v10, v14\n"
      "v_max_f32 v14, 0, v14\n"
      "v_add_f32 v14, 0x3727c5ac, v14\n"
      "v_rsq_f32 v14, v14\n"
      "s_nop 0\n"
      "v_mul_f32 v10, v10, v14\n"
      "v_pk_fma_f32 v[70:71], v[70:71], v[14:15], v[10:11] op_sel_hi:[1,0,0]\n"
      "v_pk_fma_f32 v[72:73], v[72:73], v[14:15], v[10:11] op_sel_hi:[1,0,0]\n"
      "v_pk_fma_f32 v[74:75], v[74:75], v[14:15], v[10:11] op_sel_hi:[1,0,0]\n"
      "v_pk_fma_f32 v[76:77], v[76:77], v[14:15], v[10:11] op_sel_hi:[1,0,0]\n"
      "v_cvt_pk_bf16_f32 v16, v70, v71\n"
      "v_cvt_pk_bf16_f32 v17, v72, v73\n"
      "v_cvt_pk_bf16_f32 v18, v74, v75\n"
      "v_cvt_pk_bf16_f32 v19, v76, v77\n"
      "ds_write_b128 v52, v[16:19] offset:32768\n"
      "v_lshlrev_b32 v78, 16, v20\n"
      "v_and_b32 v79, 0xffff0000, v20\n"
      "v_lshlrev_b32 v80, 16, v21\n"
      "v_and_b32 v81, 0xffff0000, v21\n"
      "v_lshlrev_b32 v82, 16, v22\n"
      "v_and_b32 v83, 0xffff0000, v22\n"
      "v_lshlrev_b32 v84, 16, v23\n"
      "v_and_b32 v85, 0xffff0000, v23\n"
      "v_pk_fma_f32 v[78:79], v[78:79], v[14:15], v[10:11] op_sel_hi:[1,0,0]\n"
      "v_pk_fma_f32 v[80:81], v[80:81], v[14:15], v[10:11] op_sel_hi:[1,0,0]\n"
      "v_pk_fma_f32 v[82:83], v[82:83], v[14:15], v[10:11] op_sel_hi:[1,0,0]\n"
      "v_pk_fma_f32 v[84:85], v[84:85], v[14:15], v[10:11] op_sel_hi:[1,0,0]\n"
      "v_cvt_pk_bf16_f32 v20, v78, v79\n"
      "v_cvt_pk_bf16_f32 v21, v80, v81\n"
      "v_cvt_pk_bf16_f32 v22, v82, v83\n"
      "v_cvt_pk_bf16_f32 v23, v84, v85\n"
      "ds_write_b128 v53, v[20:23] offset:32768\n"
      "v_lshlrev_b32 v70, 16, v24\n"
      "v_and_b32 v71, 0xffff0000, v24\n"
      "v_lshlrev_b32 v72, 16, v25\n"
      "v_and_b32 v73, 0xffff0000, v25\n"
      "v_lshlrev_b32 v74, 16, v26\n"
      "v_and_b32 v75, 0xffff0000, v26\n"
      "v_lshlrev_b32 v76, 16, v27\n"
      "v_and_b32 v77, 0xffff0000, v27\n"
      "v_pk_fma_f32 v[70:71], v[70:71], v[14:15], v[10:11] op_sel_hi:[1,0,0]\n"
      "v_pk_fma_f32 v[72:73], v[72:73], v[14:15], v[10:11] op_sel_hi:[1,0,0]\n"
      "v_pk_fma_f32 v[74:75], v[74:75], v[14:15], v[10:11] op_sel_hi:[1,0,0]\n"
      "v_pk_fma_f32 v[76:77], v[76:77], v[14:15], v[10:11] op_sel_hi:[1,0,0]\n"
      "v_cvt_pk_bf16_f32 v24, v70, v71\n"
      "v_cvt_pk_bf16_f32 v25, v72, v73\n"
      "v_cvt_pk_bf16_f32 v26, v74, v75\n"
      "v_cvt_pk_bf16_f32 v27, v76, v77\n"
      "ds_write_b128 v54, v[24:27] offset:32768\n"
      "v_lshlrev_b32 v78, 16, v28\n"
      "v_and_b32 v79, 0xffff0000, v28\n"
      "v_lshlrev_b32 v80, 16, v29\n"
      "v_and_b32 v81, 0xffff0000, v29\n"
      "v_lshlrev_b32 v82, 16, v30\n"
      "v_and_b32 v83, 0xffff0000, v30\n"
      "v_lshlrev_b32 v84, 16, v31\n"
      "v_and_b32 v85, 0xffff0000, v31\n"
      "v_pk_fma_f32 v[78:79], v[78:79], v[14:15], v[10:11] op_sel_hi:[1,0,0]\n"
      "v_pk_fma_f32 v[80:81], v[80:81], v[14:15], v[10:11] op_sel_hi:[1,0,0]\n"
      "v_pk_fma_f32 v[82:83], v[82:83], v[14:15], v[10:11] op_sel_hi:[1,0,0]\n"
      "v_pk_fma_f32 v[84:85], v[84:85], v[14:15], v[10:11] op_sel_hi:[1,0,0]\n"
      "v_cvt_pk_bf16_f32 v28, v78, v79\n"
      "v_cvt_pk_bf16_f32 v29, v80, v81\n"
      "v_cvt_pk_bf16_f32 v30, v82, v83\n"
      "v_cvt_pk_bf16_f32 v31, v84, v85\n"
      "ds_write_b128 v55, v[28:31] offset:32768\n"
      "v_lshlrev_b32 v70, 16, v32\n"
      "v_and_b32 v71, 0xffff0000, v32\n"
      "v_lshlrev_b32 v72, 16, v33\n"
      "v_and_b32 v73, 0xffff0000, v33\n"
      "v_lshlrev_b32 v74, 16, v34\n"
      "v_and_b32 v75, 0xffff0000, v34\n"
      "v_lshlrev_b32 v76, 16, v35\n"
      "v_and_b32 v77, 0xffff0000, v35\n"
      "v_pk_fma_f32 v[70:71], v[70:71], v[14:15], v[10:11] op_sel_hi:[1,0,0]\n"
      "v_pk_fma_f32 v[72:73], v[72:73], v[14:15], v[10:11] op_sel_hi:[1,0,0]\n"
      "v_pk_fma_f32 v[74:75], v[74:75], v[14:15], v[10:11] op_sel_hi:[1,0,0]\n"
      "v_pk_fma_f32 v[76:77], v[76:77], v[14:15], v[10:11] op_sel_hi:[1,0,0]\n"
      "v_cvt_pk_bf16_f32 v32, v70, v71\n"
      "v_cvt_pk_bf16_f32 v33, v72, v73\n"
      "v_cvt_pk_bf16_f32 v34, v74, v75\n"
      "v_cvt_pk_bf16_f32 v35, v76, v77\n"
      "ds_write_b128 v56, v[32:35] offset:32768\n"
      "v_lshlrev_b32 v78, 16, v36\n"
      "v_and_b32 v79, 0xffff0000, v36\n"
      "v_lshlrev_b32 v80, 16, v37\n"
      "v_and_b32 v81, 0xffff0000, v37\n"
      "v_lshlrev_b32 v82, 16, v38\n"
      "v_and_b32 v83, 0xffff0000, v38\n"
      "v_lshlrev_b32 v84, 16, v39\n"
      "v_and_b32 v85, 0xffff0000, v39\n"
      "v_pk_fma_f32 v[78:79], v[78:79], v[14:15], v[10:11] op_sel_hi:[1,0,0]\n"
      "v_pk_fma_f32 v[80:81], v[80:81], v[14:15], v[10:11] op_sel_hi:[1,0,0]\n"
      "v_pk_fma_f32 v[82:83], v[82:83], v[14:15], v[10:11] op_sel_hi:[1,0,0]\n"
      "v_pk_fma_f32 v[84:85], v[84:85], v[14:15], v[10:11] op_sel_hi:[1,0,0]\n"
      "v_cvt_pk_bf16_f32 v36, v78, v79\n"
      "v_cvt_pk_bf16_f32 v37, v80, v81\n"
      "v_cvt_pk_bf16_f32 v38, v82, v83\n"
      "v_cvt_pk_bf16_f32 v39, v84, v85\n"
      "ds_write_b128 v57, v[36:39] offset:32768\n"
      "v_lshlrev_b32 v70, 16, v40\n"
      "v_and_b32 v71, 0xffff0000, v40\n"
      "v_lshlrev_b32 v72, 16, v41\n"
      "v_and_b32 v73, 0xffff0000, v41\n"
      "v_lshlrev_b32 v74, 16, v42\n"
      "v_and_b32 v75, 0xffff0000, v42\n"
      "v_lshlrev_b32 v76, 16, v43\n"
      "v_and_b32 v77, 0xffff0000, v43\n"
      "v_pk_fma_f32 v[70:71], v[70:71], v[14:15], v[10:11] op_sel_hi:[1,0,0]\n"
      "v_pk_fma_f32 v[72:73], v[72:73], v[14:15], v[10:11] op_sel_hi:[1,0,0]\n"
      "v_pk_fma_f32 v[74:75], v[74:75], v[14:15], v[10:11] op_sel_hi:[1,0,0]\n"
      "v_pk_fma_f32 v[76:77], v[76:77], v[14:15], v[10:11] op_sel_hi:[1,0,0]\n"
      "v_cvt_pk_bf16_f32 v40, v70, v71\n"
      "v_cvt_pk_bf16_f32 v41, v72, v73\n"
      "v_cvt_pk_bf16_f32 v42, v74, v75\n"
      "v_cvt_pk_bf16_f32 v43, v76, v77\n"
      "ds_write_b128 v58, v[40:43] offset:32768\n"
      "v_lshlrev_b32 v78, 16, v44\n"
      "v_and_b32 v79, 0xffff0000, v44\n"
      "v_lshlrev_b32 v80, 16, v45\n"
      "v_and_b32 v81, 0xffff0000, v45\n"
      "v_lshlrev_b32 v82, 16, v46\n"
      "v_and_b32 v83, 0xffff0000, v46\n"
      "v_lshlrev_b32 v84, 16, v47\n"
      "v_and_b32 v85, 0xffff0000, v47\n"
      "v_pk_fma_f32 v[78:79], v[78:79], v[14:15], v[10:11] op_sel_hi:[1,0,0]\n"
      "v_pk_fma_f32 v[80:81], v[80:81], v[14:15], v[10:11] op_sel_hi:[1,0,0]\n"
      "v_pk_fma_f32 v[82:83], v[82:83], v[14:15], v[10:11] op_sel_hi:[1,0,0]\n"
      "v_pk_fma_f32 v[84:85], v[84:85], v[14:15], v[10:11] op_sel_hi:[1,0,0]\n"
      "v_cvt_pk_bf16_f32 v44, v78, v79\n"
      "v_cvt_pk_bf16_f32 v45, v80, v81\n"
      "v_cvt_pk_bf16_f32 v46, v82, v83\n"
      "v_cvt_pk_bf16_f32 v47, v84, v85\n"
      "ds_write_b128 v50, v[44:47] offset:32768\n"
      "s_setprio 0\n"
      "s_waitcnt vmcnt(1)\n"
      "s_waitcnt lgkmcnt(0)\n"
      "s_barrier\n"
      "s_load_dwordx2 s[4:5], s[28:29], 0x3c\n"
      "s_load_dwordx2 s[4:5], s[28:29], 0xbc\n"
      "s_load_dwordx2 s[4:5], s[28:29], 0x13c\n"
      "s_load_dwordx2 s[4:5], s[28:29], 0x1bc\n"
      "s_load_dwordx2 s[4:5], s[30:31], 0x3c\n"
      "s_load_dwordx2 s[4:5], s[30:31], 0xbc\n"
      "s_load_dwordx2 s[4:5], s[30:31], 0x13c\n"
      "s_barrier\n"
      "s_barrier\n"
      "s_add_u32 m0, s77, 31744\n"
      "s_nop 0\n"
      "buffer_load_dwordx4 v13, s[8:11], s69 offen offset:1024 lds\n"
      "buffer_load_dwordx4 v13, s[8:11], s69 offen offset:2048 lds\n"
      "s_add_u32 m0, s77, 33792\n"
      "s_nop 0\n"
      "buffer_load_dwordx4 v48, s[8:11], s69 offen offset:1024 lds\n"
      "buffer_load_dwordx4 v48, s[8:11], s69 offen offset:2048 lds\n"
      "s_barrier\n"
      "s_add_u32 m0, s77, 48128\n"
      "s_nop 0\n"
      "buffer_load_dwordx4 v13, s[8:11], s70 offen offset:1024 lds\n"
      "buffer_load_dwordx4 v13, s[8:11], s70 offen offset:2048 lds\n"
      "s_add_u32 m0, s77, 50176\n"
      "s_nop 0\n"
      "buffer_load_dwordx4 v48, s[8:11], s70 offen offset:1024 lds\n"
      "buffer_load_dwordx4 v48, s[8:11], s70 offen offset:2048 lds\n"
      "s_barrier\n"
      "s_add_u32 m0, s77, 64512\n"
      "s_nop 0\n"
      "buffer_load_dwordx4 v13, s[8:11], s71 offen offset:1024 lds\n"
      "buffer_load_dwordx4 v13, s[8:11], s71 offen offset:2048 lds\n"
      "s_add_u32 m0, s77, 66560\n"
      "s_nop 0\n"
      "buffer_load_dwordx4 v48, s[8:11], s71 offen offset:1024 lds\n"
      "buffer_load_dwordx4 v48, s[8:11], s71 offen offset:2048 lds\n"
      "s_waitcnt vmcnt(8)\n"
      "s_barrier\n"
      "s_add_u32 m0, s77, 0\n"
      "s_nop 0\n"
      "buffer_load_dwordx4 v13, s[8:11], s64 offen lds\n"
      "s_add_u32 m0, s77, 2048\n"
      "s_nop 0\n"
      "buffer_load_dwordx4 v48, s[8:11], s64 offen lds\n"
      "s_add_u32 m0, s77, 1024\n"
      "s_nop 0\n"
      "buffer_load_dwordx4 v13, s[8:11], s65 offen lds\n"
      "s_add_u32 m0, s77, 3072\n"
      "s_nop 0\n"
      "buffer_load_dwordx4 v48, s[8:11], s65 offen lds\n"
      "s_add_u32 s72, s39, s48\n"
      "s_sub_u32 s72, s72, 4\n"
      "s_cselect_b32 s53, 1073741824, 0\n"
      "s_cselect_b32 s72, 0, s72\n"
      "v_add_u32 v65, s53, v2\n"
      "s_lshr_b32 s53, s72, 1\n"
      ".Lpage1_%=:\n"
      "s_cbranch_execz .Lret_%=\n"
      "s_lshl_b32 s52, s43, 7\n"
      "s_add_u32 s52, s52, 33558528\n"
      "buffer_load_dwordx2 v[8:9], v64, s[24:27], s52 offen sc1\n"
      "s_lshl_b32 s54, s53, 7\n"
      "s_add_u32 s54, s54, 33558528\n"
      "buffer_load_dwordx2 v[10:11], v64, s[24:27], s54 offen sc1\n"
      "s_load_dwordx2 s[4:5], s[30:31], 0x1bc\n"
      "s_load_dwordx2 s[4:5], s[30:31], 0x23c\n"
      "s_load_dwordx2 s[4:5], s[30:31], 0x2bc\n"
      "s_load_dwordx2 s[4:5], s[30:31], 0x33c\n"
      "s_load_dwordx2 s[4:5], s[30:31], 0x3bc\n"
      "s_load_dwordx2 s[4:5], s[30:31], 0x43c\n"
      "s_load_dwordx2 s[4:5], s[30:31], 0x4bc\n"
      "s_waitcnt vmcnt(10)\n"
      "s_barrier\n"
      "s_add_u32 m0, s77, 16384\n"
      "s_nop 0\n"
      "buffer_load_dwordx4 v13, s[8:11], s66 offen lds\n"
      "s_add_u32 m0, s77, 18432\n"
      "s_nop 0\n"
      "buffer_load_dwordx4 v48, s[8:11], s66 offen lds\n"
      "s_add_u32 m0, s77, 17408\n"
      "s_nop 0\n"
      "buffer_load_dwordx4 v13, s[8:11], s67 offen lds\n"
      "s_add_u32 m0, s77, 19456\n"
      "s_nop 0\n"
      "buffer_load_dwordx4 v48, s[8:11], s67 offen lds\n"
      "s_waitcnt vmcnt(10)\n"
      "s_barrier\n"
      "s_add_u32 m0, s77, 32768\n"
      "s_nop 0\n"
      "buffer_load_dwordx4 v13, s[8:11], s68 offen lds\n"
      "s_add_u32 m0, s77, 34816\n"
      "s_nop 0\n"
      "buffer_load_dwordx4 v48, s[8:11], s68 offen lds\n"
      "s_add_u32 m0, s77, 33792\n"
      "s_nop 0\n"
      "buffer_load_dwordx4 v13, s[8:11], s69 offen lds\n"
      "s_add_u32 m0, s77, 35840\n"
      "s_nop 0\n"
      "buffer_load_dwordx4 v48, s[8:11], s69 offen lds\n"
      "s_waitcnt vmcnt(10)\n"
      "s_barrier\n"
      "s_add_u32 m0, s77, 49152\n"
      "s_nop 0\n"
      "buffer_load_dwordx4 v13, s[8:11], s70 offen lds\n"
      "s_add_u32 m0, s77, 51200\n"
      "s_nop 0\n"
      "buffer_load_dwordx4 v48, s[8:11], s70 offen lds\n"
      "s_add_u32 m0, s77, 50176\n"
      "s_nop 0\n"
      "buffer_load_dwordx4 v13, s[8:11], s71 offen lds\n"
      "s_add_u32 m0, s77, 52224\n"
      "s_nop 0\n"
      "buffer_load_dwordx4 v48, s[8:11], s71 offen lds\n"
      "s_waitcnt vmcnt(8)\n"
      "s_barrier\n"
      "s_mov_b32 s60, 0x100000\n"
      "v_readfirstlane_b32 s58, v8\n"
      ".Lann_1002_%=:\n"
      "v_readfirstlane_b32 s59, v9\n"
      "s_nop 1\n"
      "s_xor_b64 s[58:59], s[58:59], s[36:37]\n"
      "s_and_b32 s76, s58, 1\n"
      "s_lshr_b64 s[58:59], s[58:59], 1\n"
      "s_cbranch_scc0 .Lannend_1003_%=\n"
      "buffer_load_dwordx2 v[8:9], v64, s[24:27], s52 offen sc1\n"
      "s_waitcnt vmcnt(0)\n"
      "v_readfirstlane_b32 s58, v8\n"
      "s_sub_u32 s60, s60, 1\n"
      "s_cbranch_scc0 .Lann_1002_%=\n"
      ".Lannend_1003_%=:\n"
      "s_lshl_b32 s76, s76, 13\n"
      "s_lshl_b32 s59, s43, 21\n"
      "s_add_u32 s76, s76, s59\n"
      "s_mov_b32 s60, 0x100000\n"
      "v_readfirstlane_b32 s58, v10\n"
      ".Lann_1004_%=:\n"
      "v_readfirstlane_b32 s59, v11\n"
      "s_nop 1\n"
      "s_xor_b64 s[58:59], s[58:59], s[36:37]\n"
      "s_and_b32 s55, s58, 1\n"
      "s_lshr_b64 s[58:59], s[58:59], 1\n"
      "s_cbranch_scc0 .Lannend_1005_%=\n"
      "buffer_load_dwordx2 v[10:11], v64, s[24:27], s54 offen sc1\n"
      "s_waitcnt vmcnt(0)\n"
      "v_readfirstlane_b32 s58, v10\n"
      "s_sub_u32 s60, s60, 1\n"
      "s_cbranch_scc0 .Lann_1004_%=\n"
      ".Lannend_1005_%=:\n"
      "s_lshl_b32 s55, s55, 13\n"
      "s_lshl_b32 s59, s53, 21\n"
      "s_add_u32 s55, s55, s59\n"
      "s_and_b32 s72, s72, 1\n"
      "s_lshl_b32 s61, s72, 10\n"
      "s_lshl_b32 s59, s40, 7\n"
      "s_add_u32 s61, s61, s59\n"
      "s_add_u32 s61, s61, s55\n"
      "s_add_u32 s61, s61, 1081344\n"
      "s_lshl_b32 s52, s72, 17\n"
      "s_lshl_b32 s59, s40, 13\n"
      "s_add_u32 s52, s52, s59\n"
      "s_and_b32 s59, s52, 0xffffe000\n"
      "s_add_u32 s52, s52, s59\n"
      "s_add_u32 s52, s52, s55\n"
      "s_lshl_b32 s54, s48, 14\n"
      "s_waitcnt vmcnt(4)\n"
      "s_barrier\n"
      "s_waitcnt vmcnt(0)\n"
      "s_barrier\n"
      "s_add_u32 s53, s39, s48\n"
      "s_cmp_lt_u32 s53, 4\n"
      "s_cbranch_scc1 .Lhalogo_1006_%=\n"
      "s_add_u32 s6, s24, s61\n"
      "s_addc_u32 s7, s25, 0\n"
      "s_mov_b32 s60, 0x100001\n"
      ".Lspoll_1007_%=:\n"
      "s_load_dwordx2 s[94:95], s[6:7], 0x0 glc\n"
      ".Lscheck_1008_%=:\n"
      "s_sub_u32 s60, s60, 1\n"
      "s_waitcnt lgkmcnt(0)\n"
      "s_cbranch_scc1 .Lspollend_1009_%=\n"
      "s_cmp_eq_u64 s[94:95], s[36:37]\n"
      "s_cbranch_scc0 .Lspoll_1007_%=\n"
      ".Lspollend_1009_%=:\n"
      ".Lhalogo_1006_%=:\n"
      "s_barrier\n"
      "s_add_u32 s53, s52, 131072\n"
      "s_add_u32 m0, s54, 73728\n"
      "s_nop 0\n"
      "buffer_load_dwordx4 v65, s[24:27], s53 offen sc1 lds\n"
      "buffer_load_dwordx4 v65, s[24:27], s53 offen offset:1024 sc1 lds\n"
      "buffer_load_dwordx4 v65, s[24:27], s53 offen offset:2048 sc1 lds\n"
      "buffer_load_dwordx4 v65, s[24:27], s53 offen offset:3072 sc1 lds\n"
      "s_add_u32 s53, s52, 135168\n"
      "s_add_u32 m0, s54, 77824\n"
      "s_nop 0\n"
      "buffer_load_dwordx4 v65, s[24:27], s53 offen sc1 lds\n"
      "buffer_load_dwordx4 v65, s[24:27], s53 offen offset:1024 sc1 lds\n"
      "buffer_load_dwordx4 v65, s[24:27], s53 offen offset:2048 sc1 lds\n"
      "buffer_load_dwordx4 v65, s[24:27], s53 offen offset:3072 sc1 lds\n"
      "s_add_u32 s53, s52, 0\n"
      "s_add_u32 m0, s54, 65536\n"
      "s_nop 0\n"
      "buffer_load_dwordx4 v65, s[24:27], s53 offen sc1 lds\n"
      "buffer_load_dwordx4 v65, s[24:27], s53 offen offset:1024 sc1 lds\n"
      "buffer_load_dwordx4 v65, s[24:27], s53 offen offset:2048 sc1 lds\n"
      "buffer_load_dwordx4 v65, s[24:27], s53 offen offset:3072 sc1 lds\n"
      "s_waitcnt vmcnt(4)\n"
      "s_barrier\n"
      "s_load_dwordx2 s[4:5], s[30:31], 0x53c\n"
      "s_load_dwordx2 s[4:5], s[88:89], 0x3c\n"
      "s_load_dwordx2 s[4:5], s[88:89], 0xbc\n"
      "s_load_dwordx2 s[4:5], s[88:89], 0x13c\n"
      "s_load_dwordx2 s[4:5], s[88:89], 0x1bc\n"
      "s_add_u32 s53, s52, 4096\n"
      "s_add_u32 m0, s54, 69632\n"
      "s_nop 0\n"
      "buffer_load_dwordx4 v65, s[24:27], s53 offen sc1 lds\n"
      "buffer_load_dwordx4 v65, s[24:27], s53 offen offset:1024 sc1 lds\n"
      "buffer_load_dwordx4 v65, s[24:27], s53 offen offset:2048 sc1 lds\n"
      "buffer_load_dwordx4 v65, s[24:27], s53 offen offset:3072 sc1 lds\n"
      "global_load_ushort v68, v68, s[92:93]\n"
      "s_mul_i32 s57, s78, 16\n"
      "s_lshr_b32 s57, s57, 3\n"
      "s_add_u32 s57, s57, 0\n"
      "s_sub_u32 s59, s57, 16\n"
      "s_cselect_b32 s57, s57, s59\n"
      "s_lshl_b32 s56, s57, 14\n"
      "s_lshl_b32 s59, s50, 18\n"
      "s_add_u32 s56, s56, s59\n"
      "s_lshl_b32 s57, s57, 11\n"
      "s_lshl_b32 s59, s50, 13\n"
      "s_add_u32 s57, s57, s59\n"
      "s_add_u32 m0, s57, s62\n"
      "s_nop 0\n"
      "buffer_load_dwordx4 v62, s[12:15], s56 offen lds\n"
      "s_mul_i32 s57, s78, 16\n"
      "s_lshr_b32 s57, s57, 3\n"
      "s_add_u32 s57, s57, 1\n"
      "s_sub_u32 s59, s57, 16\n"
      "s_cselect_b32 s57, s57, s59\n"
      "s_lshl_b32 s56, s57, 14\n"
      "s_lshl_b32 s59, s50, 18\n"
      "s_add_u32 s56, s56, s59\n"
      "s_lshl_b32 s57, s57, 11\n"
      "s_lshl_b32 s59, s50, 13\n"
      "s_add_u32 s57, s57, s59\n"
      "s_add_u32 m0, s57, s62\n"
      "s_nop 0\n"
      "buffer_load_dwordx4 v62, s[12:15], s56 offen lds\n"
      "s_mul_i32 s57, s78, 16\n"
      "s_lshr_b32 s57, s57, 3\n"
      "s_add_u32 s57, s57, 2\n"
      "s_sub_u32 s59, s57, 16\n"
      "s_cselect_b32 s57, s57, s59\n"
      "s_lshl_b32 s56, s57, 14\n"
      "s_lshl_b32 s59, s50, 18\n"
      "s_add_u32 s56, s56, s59\n"
      "s_lshl_b32 s57, s57, 11\n"
      "s_lshl_b32 s59, s50, 13\n"
      "s_add_u32 s57, s57, s59\n"
      "s_add_u32 m0, s57, s62\n"
      "s_nop 0\n"
      "buffer_load_dwordx4 v62, s[12:15], s56 offen lds\n"
      "s_mul_i32 s57, s78, 16\n"
      "s_lshr_b32 s57, s57, 3\n"
      "s_add_u32 s57, s57, 3\n"
      "s_sub_u32 s59, s57, 16\n"
      "s_cselect_b32 s57, s57, s59\n"
      "s_lshl_b32 s56, s57, 14\n"
      "s_lshl_b32 s59, s50, 18\n"
      "s_add_u32 s56, s56, s59\n"
      "s_lshl_b32 s57, s57, 11\n"
      "s_lshl_b32 s59, s50, 13\n"
      "s_add_u32 s57, s57, s59\n"
      "s_add_u32 m0, s57, s62\n"
      "s_nop 0\n"
      "buffer_load_dwordx4 v62, s[12:15], s56 offen lds\n"
      "s_lshl_b32 s58, s48, 2\n"
      "s_lshr_b32 s58, 0x8876, s58\n"
      "s_and_b32 s58, s58, 15\n"
      "s_min_u32 s58, s58, 8\n"
      "s_lshl_b32 s58, s58, 12\n"
      "s_getpc_b64 s[56:57]\n"
      ".Lit_1010_%=:\n"
      "s_sub_u32 s56, s56, .Lit_1010_%=-.Lpage1_%=+4096\n"
      "s_subb_u32 s57, s57, 0\n"
      "s_add_u32 s56, s56, s58\n"
      "s_addc_u32 s57, s57, 0\n"
      "s_mov_b64 exec, 0\n"
      "s_swappc_b64 s[58:59], s[56:57]\n"
      "s_mov_b64 exec, -1\n"
      "s_waitcnt vmcnt(5)\n"
      "s_barrier\n"
      "s_load_dwordx2 s[4:5], s[88:89], 0x23c\n"
      "s_load_dwordx2 s[4:5], s[88:89], 0x2bc\n"
      "s_load_dwordx2 s[4:5], s[88:89], 0x33c\n"
      "s_load_dwordx2 s[4:5], s[88:89], 0x3bc\n"
      "s_load_dwordx2 s[4:5], s[88:89], 0x43c\n"
      "s_load_dwordx2 s[4:5], s[88:89], 0x4bc\n"
      "s_load_dwordx2 s[4:5], s[88:89], 0x53c\n"
      "s_mul_i32 s57, s78, 16\n"
      "s_lshr_b32 s57, s57, 3\n"
      "s_add_u32 s57, s57, 4\n"
      "s_sub_u32 s59, s57, 16\n"
      "s_cselect_b32 s57, s57, s59\n"
      "s_lshl_b32 s56, s57, 14\n"
      "s_lshl_b32 s59, s50, 18\n"
      "s_add_u32 s56, s56, s59\n"
      "s_lshl_b32 s57, s57, 11\n"
      "s_lshl_b32 s59, s50, 13\n"
      "s_add_u32 s57, s57, s59\n"
      "s_add_u32 m0, s57, s62\n"
      "s_nop 0\n"
      "buffer_load_dwordx4 v62, s[12:15], s56 offen lds\n"
      "s_mul_i32 s57, s78, 16\n"
      "s_lshr_b32 s57, s57, 3\n"
      "s_add_u32 s57, s57, 5\n"
      "s_sub_u32 s59, s57, 16\n"
      "s_cselect_b32 s57, s57, s59\n"
      "s_lshl_b32 s56, s57, 14\n"
      "s_lshl_b32 s59, s50, 18\n"
      "s_add_u32 s56, s56, s59\n"
      "s_lshl_b32 s57, s57, 11\n"
      "s_lshl_b32 s59, s50, 13\n"
      "s_add_u32 s57, s57, s59\n"
      "s_add_u32 m0, s57, s62\n"
      "s_nop 0\n"
      "buffer_load_dwordx4 v62, s[12:15], s56 offen lds\n"
      "s_mul_i32 s57, s78, 16\n"
      "s_lshr_b32 s57, s57, 3\n"
      "s_add_u32 s57, s57, 6\n"
      "s_sub_u32 s59, s57, 16\n"
      "s_cselect_b32 s57, s57, s59\n"
      "s_lshl_b32 s56, s57, 14\n"
      "s_lshl_b32 s59, s50, 18\n"
      "s_add_u32 s56, s56, s59\n"
      "s_lshl_b32 s57, s57, 11\n"
      "s_lshl_b32 s59, s50, 13\n"
      "s_add_u32 s57, s57, s59\n"
      "s_add_u32 m0, s57, s62\n"
      "s_nop 0\n"
      "buffer_load_dwordx4 v62, s[12:15], s56 offen lds\n"
      "s_mul_i32 s57, s78, 16\n"
      "s_lshr_b32 s57, s57, 3\n"
      "s_add_u32 s57, s57, 7\n"
      "s_sub_u32 s59, s57, 16\n"
      "s_cselect_b32 s57, s57, s59\n"
      "s_lshl_b32 s56, s57, 14\n"
      "s_lshl_b32 s59, s50, 18\n"
      "s_add_u32 s56, s56, s59\n"
      "s_lshl_b32 s57, s57, 11\n"
      "s_lshl_b32 s59, s50, 13\n"
      "s_add_u32 s57, s57, s59\n"
      "s_add_u32 m0, s57, s62\n"
      "s_nop 0\n"
      "buffer_load_dwordx4 v62, s[12:15], s56 offen lds\n"
      "s_mul_i32 s57, s78, 16\n"
      "s_lshr_b32 s57, s57, 3\n"
      "s_add_u32 s57, s57, 8\n"
      "s_sub_u32 s59, s57, 16\n"
      "s_cselect_b32 s57, s57, s59\n"
      "s_lshl_b32 s56, s57, 14\n"
      "s_lshl_b32 s59, s50, 18\n"
      "s_add_u32 s56, s56, s59\n"
      "s_lshl_b32 s57, s57, 11\n"
      "s_lshl_b32 s59, s50, 13\n"
      "s_add_u32 s57, s57, s59\n"
      "s_add_u32 m0, s57, s62\n"
      "s_nop 0\n"
      "buffer_load_dwordx4 v62, s[12:15], s56 offen lds\n"
      "s_mul_i32 s57, s78, 16\n"
      "s_lshr_b32 s57, s57, 3\n"
      "s_add_u32 s57, s57, 9\n"
      "s_sub_u32 s59, s57, 16\n"
      "s_cselect_b32 s57, s57, s59\n"
      "s_lshl_b32 s56, s57, 14\n"
      "s_lshl_b32 s59, s50, 18\n"
      "s_add_u32 s56, s56, s59\n"
      "s_lshl_b32 s57, s57, 11\n"
      "s_lshl_b32 s59, s50, 13\n"
      "s_add_u32 s57, s57, s59\n"
      "s_add_u32 m0, s57, s62\n"
      "s_nop 0\n"
      "buffer_load_dwordx4 v62, s[12:15], s56 offen lds\n"
      "s_mul_i32 s57, s78, 16\n"
      "s_lshr_b32 s57, s57, 3\n"
      "s_add_u32 s57, s57, 10\n"
      "s_sub_u32 s59, s57, 16\n"
      "s_cselect_b32 s57, s57, s59\n"
      "s_lshl_b32 s56, s57, 14\n"
      "s_lshl_b32 s59, s50, 18\n"
      "s_add_u32 s56, s56, s59\n"
      "s_lshl_b32 s57, s57, 11\n"
      "s_lshl_b32 s59, s50, 13\n"
      "s_add_u32 s57, s57, s59\n"
      "s_add_u32 m0, s57, s62\n"
      "s_nop 0\n"
      "buffer_load_dwordx4 v62, s[12:15], s56 offen lds\n"
      "s_mul_i32 s57, s78, 16\n"
      "s_lshr_b32 s57, s57, 3\n"
      "s_add_u32 s57, s57, 11\n"
      "s_sub_u32 s59, s57, 16\n"
      "s_cselect_b32 s57, s57, s59\n"
      "s_lshl_b32 s56, s57, 14\n"
      "s_lshl_b32 s59, s50, 18\n"
      "s_add_u32 s56, s56, s59\n"
      "s_lshl_b32 s57, s57, 11\n"
      "s_lshl_b32 s59, s50, 13\n"
      "s_add_u32 s57, s57, s59\n"
      "s_add_u32 m0, s57, s62\n"
      "s_nop 0\n"
      "buffer_load_dwordx4 v62, s[12:15], s56 offen lds\n"
      "s_mul_i32 s57, s78, 16\n"
      "s_lshr_b32 s57, s57, 3\n"
      "s_add_u32 s57, s57, 12\n"
      "s_sub_u32 s59, s57, 16\n"
      "s_cselect_b32 s57, s57, s59\n"
      "s_lshl_b32 s56, s57, 14\n"
      "s_lshl_b32 s59, s50, 18\n"
      "s_add_u32 s56, s56, s59\n"
      "s_lshl_b32 s57, s57, 11\n"
      "s_lshl_b32 s59, s50, 13\n"
      "s_add_u32 s57, s57, s59\n"
      "s_add_u32 m0, s57, s62\n"
      "s_nop 0\n"
      "buffer_load_dwordx4 v62, s[12:15], s56 offen lds\n"
      "s_mul_i32 s57, s78, 16\n"
      "s_lshr_b32 s57, s57, 3\n"
      "s_add_u32 s57, s57, 13\n"
      "s_sub_u32 s59, s57, 16\n"
      "s_cselect_b32 s57, s57, s59\n"
      "s_lshl_b32 s56, s57, 14\n"
      "s_lshl_b32 s59, s50, 18\n"
      "s_add_u32 s56, s56, s59\n"
      "s_lshl_b32 s57, s57, 11\n"
      "s_lshl_b32 s59, s50, 13\n"
      "s_add_u32 s57, s57, s59\n"
      "s_add_u32 m0, s57, s62\n"
      "s_nop 0\n"
      "buffer_load_dwordx4 v62, s[12:15], s56 offen lds\n"
      "s_mul_i32 s57, s78, 16\n"
      "s_lshr_b32 s57, s57, 3\n"
      "s_add_u32 s57, s57, 14\n"
      "s_sub_u32 s59, s57, 16\n"
      "s_cselect_b32 s57, s57, s59\n"
      "s_lshl_b32 s56, s57, 14\n"
      "s_lshl_b32 s59, s50, 18\n"
      "s_add_u32 s56, s56, s59\n"
      "s_lshl_b32 s57, s57, 11\n"
      "s_lshl_b32 s59, s50, 13\n"
      "s_add_u32 s57, s57, s59\n"
      "s_add_u32 m0, s57, s62\n"
      "s_nop 0\n"
      "buffer_load_dwordx4 v62, s[12:15], s56 offen lds\n"
      "s_waitcnt vmcnt(15)\n"
      "s_mul_i32 s57, s78, 16\n"
      "s_lshr_b32 s57, s57, 3\n"
      "s_add_u32 s57, s57, 15\n"
      "s_sub_u32 s59, s57, 16\n"
      "s_cselect_b32 s57, s57, s59\n"
      "s_lshl_b32 s56, s57, 14\n"
      "s_lshl_b32 s59, s50, 18\n"
      "s_add_u32 s56, s56, s59\n"
      "s_lshl_b32 s57, s57, 11\n"
      "s_lshl_b32 s59, s50, 13\n"
      "s_add_u32 s57, s57, s59\n"
      "s_add_u32 m0, s57, s62\n"
      "s_nop 0\n"
      "buffer_load_dwordx4 v62, s[12:15], s56 offen lds\n"
      "s_waitcnt vmcnt(15)\n"
      "s_add_u32 s56, s74, 0\n"
      "buffer_load_dwordx4 v[22:25], v59, s[16:19], s56 offen\n"
      "s_waitcnt vmcnt(15)\n"
      "s_add_u32 s56, s74, 0\n"
      "buffer_load_dwordx4 v[26:29], v60, s[16:19], s56 offen\n"
      "s_waitcnt vmcnt(15)\n"
      "s_add_u32 s56, s74, 0\n"
      "buffer_load_dwordx4 v[30:33], v61, s[16:19], s56 offen\n"
      "s_waitcnt vmcnt(15)\n"
      "s_add_u32 s56, s74, 176128\n"
      "buffer_load_dwordx4 v[34:37], v59, s[16:19], s56 offen\n"
      "s_waitcnt vmcnt(15)\n"
      "s_add_u32 s56, s74, 176128\n"
      "buffer_load_dwordx4 v[38:41], v60, s[16:19], s56 offen\n"
      "s_waitcnt vmcnt(15)\n"
      "s_add_u32 s56, s74, 176128\n"
      "buffer_load_dwordx4 v[42:45], v61, s[16:19], s56 offen\n"
      "s_waitcnt vmcnt(15)\n"
      "s_add_u32 s56, s74, 352256\n"
      "buffer_load_dwordx4 v[50:53], v59, s[16:19], s56 offen\n"
      "s_waitcnt vmcnt(15)\n"
      "s_add_u32 s56, s74, 352256\n"
      "buffer_load_dwordx4 v[54:57], v60, s[16:19], s56 offen\n"
      "s_waitcnt vmcnt(15)\n"
      "s_add_u32 s56, s74, 352256\n"
      "buffer_load_dwordx4 v[70:73], v61, s[16:19], s56 offen\n"
      "s_waitcnt vmcnt(15)\n"
      "s_add_u32 s56, s74, 528384\n"
      "buffer_load_dwordx4 v[74:77], v59, s[16:19], s56 offen\n"
      "s_waitcnt vmcnt(15)\n"
      "s_add_u32 s56, s74, 528384\n"
      "buffer_load_dwordx4 v[78:81], v60, s[16:19], s56 offen\n"
      "s_waitcnt vmcnt(15)\n"
      "s_add_u32 s56, s74, 528384\n"
      "buffer_load_dwordx4 v[82:85], v61, s[16:19], s56 offen\n"
      "s_barrier\n"
      "s_waitcnt lgkmcnt(0)\n"
      "s_waitcnt vmcnt(15)\n"
      "s_add_u32 s56, s74, 704512\n"
      "s_add_u32 m0, s73, 49152\n"
      "s_nop 0\n"
      "buffer_load_dwordx4 v59, s[16:19], s56 offen lds\n"
      "s_waitcnt vmcnt(15)\n"
      "s_add_u32 s56, s74, 704512\n"
      "s_add_u32 m0, s73, 50176\n"
      "s_nop 0\n"
      "buffer_load_dwordx4 v60, s[16:19], s56 offen lds\n"
      "s_waitcnt vmcnt(15)\n"
      "s_add_u32 s56, s74, 704512\n"
      "s_add_u32 m0, s73, 51200\n"
      "s_nop 0\n"
      "buffer_load_dwordx4 v61, s[16:19], s56 offen lds\n"
      "s_waitcnt vmcnt(15)\n"
      "s_add_u32 s56, s74, 880640\n"
      "s_add_u32 m0, s73, 61440\n"
      "s_nop 0\n"
      "buffer_load_dwordx4 v59, s[16:19], s56 offen lds\n"
      "s_waitcnt vmcnt(15)\n"
      "s_add_u32 s56, s74, 880640\n"
      "s_add_u32 m0, s73, 62464\n"
      "s_nop 0\n"
      "buffer_load_dwordx4 v60, s[16:19], s56 offen lds\n"
      "s_waitcnt vmcnt(15)\n"
      "s_add_u32 s56, s74, 880640\n"
      "s_add_u32 m0, s73, 63488\n"
      "s_nop 0\n"
      "buffer_load_dwordx4 v61, s[16:19], s56 offen lds\n"
      "s_barrier\n"
      "s_waitcnt vmcnt(15)\n"
      "s_add_u32 s56, s74, 1056768\n"
      "s_add_u32 m0, s73, 73728\n"
      "s_nop 0\n"
      "buffer_load_dwordx4 v59, s[16:19], s56 offen lds\n"
      "s_add_u32 s58, s73, 24576\n"
      "v_add_u32 v63, s58, v2\n"
      "s_waitcnt vmcnt(12)\n"
      "ds_write_b128 v63, v[50:53] offset:0\n"
      "s_add_u32 s56, s74, 1056768\n"
      "s_add_u32 m0, s73, 74752\n"
      "s_nop 0\n"
      "buffer_load_dwordx4 v60, s[16:19], s56 offen lds\n"
      "s_add_u32 s58, s73, 25600\n"
      "v_add_u32 v63, s58, v2\n"
      "s_waitcnt vmcnt(12)\n"
      "ds_write_b128 v63, v[54:57] offset:0\n"
      "s_add_u32 s56, s74, 1056768\n"
      "s_add_u32 m0, s73, 75776\n"
      "s_nop 0\n"
      "buffer_load_dwordx4 v61, s[16:19], s56 offen lds\n"
      "s_add_u32 s58, s73, 26624\n"
      "v_add_u32 v63, s58, v2\n"
      "s_waitcnt vmcnt(12)\n"
      "ds_write_b128 v63, v[70:73] offset:0\n"
      "s_add_u32 s56, s74, 1232896\n"
      "s_add_u32 m0, s73, 86016\n"
      "s_nop 0\n"
      "buffer_load_dwordx4 v59, s[16:19], s56 offen lds\n"
      "s_add_u32 s58, s73, 36864\n"
      "v_add_u32 v63, s58, v2\n"
      "s_waitcnt vmcnt(12)\n"
      "ds_write_b128 v63, v[74:77] offset:0\n"
      "s_add_u32 s56, s74, 1232896\n"
      "s_add_u32 m0, s73, 87040\n"
      "s_nop 0\n"
      "buffer_load_dwordx4 v60, s[16:19], s56 offen lds\n"
      "s_add_u32 s56, s74, 1232896\n"
      "s_add_u32 m0, s73, 88064\n"
      "s_nop 0\n"
      "buffer_load_dwordx4 v61, s[16:19], s56 offen lds\n"
      "s_mul_i32 s57, s47, 7\n"
      "s_lshr_b32 s57, s57, 3\n"
      "s_add_u32 s57, s57, 0\n"
      "s_sub_u32 s59, s57, 7\n"
      "s_cselect_b32 s57, s57, s59\n"
      "s_lshl_b32 s56, s57, 14\n"
      "s_mul_i32 s59, s50, 704512\n"
      "s_add_u32 s56, s56, s59\n"
      "s_add_u32 s56, s56, 589824\n"
      "buffer_load_dwordx4 v[50:53], v62, s[20:23], s56 offen\n"
      "s_mul_i32 s57, s47, 7\n"
      "s_lshr_b32 s57, s57, 3\n"
      "s_add_u32 s57, s57, 1\n"
      "s_sub_u32 s59, s57, 7\n"
      "s_cselect_b32 s57, s57, s59\n"
      "s_lshl_b32 s56, s57, 14\n"
      "s_mul_i32 s59, s50, 704512\n"
      "s_add_u32 s56, s56, s59\n"
      "s_add_u32 s56, s56, 589824\n"
      "buffer_load_dwordx4 v[54:57], v62, s[20:23], s56 offen\n"
      "s_waitcnt vmcnt(15)\n"
      "s_mul_i32 s57, s47, 7\n"
      "s_lshr_b32 s57, s57, 3\n"
      "s_add_u32 s57, s57, 2\n"
      "s_sub_u32 s59, s57, 7\n"
      "s_cselect_b32 s57, s57, s59\n"
      "s_lshl_b32 s56, s57, 14\n"
      "s_mul_i32 s59, s50, 704512\n"
      "s_add_u32 s56, s56, s59\n"
      "s_add_u32 s56, s56, 589824\n"
      "buffer_load_dwordx4 v[70:73], v62, s[20:23], s56 offen\n"
      "s_waitcnt vmcnt(15)\n"
      "s_mul_i32 s57, s47, 7\n"
      "s_lshr_b32 s57, s57, 3\n"
      "s_add_u32 s57, s57, 3\n"
      "s_sub_u32 s59, s57, 7\n"
      "s_cselect_b32 s57, s57, s59\n"
      "s_lshl_b32 s56, s57, 14\n"
      "s_mul_i32 s59, s50, 704512\n"
      "s_add_u32 s56, s56, s59\n"
      "s_add_u32 s56, s56, 589824\n"
      "buffer_load_dwordx4 v[74:77], v62, s[20:23], s56 offen\n"
      "s_waitcnt lgkmcnt(0)\n"
      "s_barrier\n"
      "s_add_u32 s58, s73, 37888\n"
      "v_add_u32 v63, s58, v2\n"
      "ds_write_b128 v63, v[78:81] offset:0\n"
      "s_add_u32 s58, s73, 38912\n"
      "v_add_u32 v63, s58, v2\n"
      "ds_write_b128 v63, v[82:85] offset:0\n"
      "s_waitcnt lgkmcnt(0)\n"
      "s_barrier\n"
      "s_waitcnt vmcnt(15)\n"
      "s_mul_i32 s57, s47, 7\n"
      "s_lshr_b32 s57, s57, 3\n"
      "s_add_u32 s57, s57, 4\n"
      "s_sub_u32 s59, s57, 7\n"
      "s_cselect_b32 s57, s57, s59\n"
      "s_lshl_b32 s56, s57, 14\n"
      "s_mul_i32 s59, s50, 704512\n"
      "s_add_u32 s56, s56, s59\n"
      "s_add_u32 s56, s56, 589824\n"
      "buffer_load_dwordx4 v[78:81], v62, s[20:23], s56 offen\n"
      "s_waitcnt vmcnt(15)\n"
      "s_mul_i32 s57, s47, 7\n"
      "s_lshr_b32 s57, s57, 3\n"
      "s_add_u32 s57, s57, 5\n"
      "s_sub_u32 s59, s57, 7\n"
      "s_cselect_b32 s57, s57, s59\n"
      "s_lshl_b32 s56, s57, 14\n"
      "s_mul_i32 s59, s50, 704512\n"
      "s_add_u32 s56, s56, s59\n"
      "s_add_u32 s56, s56, 589824\n"
      "buffer_load_dwordx4 v[82:85], v62, s[20:23], s56 offen\n"
      "s_waitcnt vmcnt(15)\n"
      "s_mul_i32 s57, s47, 7\n"
      "s_lshr_b32 s57, s57, 3\n"
      "s_add_u32 s57, s57, 6\n"
      "s_sub_u32 s59, s57, 7\n"
      "s_cselect_b32 s57, s57, s59\n"
      "s_lshl_b32 s56, s57, 14\n"
      "s_mul_i32 s59, s50, 704512\n"
      "s_add_u32 s56, s56, s59\n"
      "s_add_u32 s56, s56, 589824\n"
      "buffer_load_dwordx4 v[86:89], v62, s[20:23], s56 offen\n"
      "s_waitcnt vmcnt(15)\n"
      "s_mul_i32 s57, s47, 12\n"
      "s_lshr_b32 s57, s57, 3\n"
      "s_add_u32 s57, s57, 0\n"
      "s_sub_u32 s59, s57, 12\n"
      "s_cselect_b32 s57, s57, s59\n"
      "s_lshl_b32 s56, s57, 14\n"
      "s_mul_i32 s59, s50, 704512\n"
      "s_add_u32 s56, s56, s59\n"
      "buffer_load_dwordx4 v[90:93], v62, s[20:23], s56 offen\n"
      "s_waitcnt vmcnt(15)\n"
      "s_mul_i32 s57, s47, 12\n"
      "s_lshr_b32 s57, s57, 3\n"
      "s_add_u32 s57, s57, 1\n"
      "s_sub_u32 s59, s57, 12\n"
      "s_cselect_b32 s57, s57, s59\n"
      "s_lshl_b32 s56, s57, 14\n"
      "s_mul_i32 s59, s50, 704512\n"
      "s_add_u32 s56, s56, s59\n"
      "buffer_load_dwordx4 v[94:97], v62, s[20:23], s56 offen\n"
      "s_waitcnt vmcnt(15)\n"
      "s_mul_i32 s57, s47, 12\n"
      "s_lshr_b32 s57, s57, 3\n"
      "s_add_u32 s57, s57, 2\n"
      "s_sub_u32 s59, s57, 12\n"
      "s_cselect_b32 s57, s57, s59\n"
      "s_lshl_b32 s56, s57, 14\n"
      "s_mul_i32 s59, s50, 704512\n"
      "s_add_u32 s56, s56, s59\n"
      "buffer_load_dwordx4 v[98:101], v62, s[20:23], s56 offen\n"
      "s_barrier\n"
      "s_add_u32 s58, s73, 0\n"
      "v_add_u32 v63, s58, v2\n"
      "ds_write_b128 v63, v[22:25] offset:0\n"
      "s_add_u32 s58, s73, 1024\n"
      "v_add_u32 v63, s58, v2\n"
      "ds_write_b128 v63, v[26:29] offset:0\n"
      "s_add_u32 s58, s73, 2048\n"
      "v_add_u32 v63, s58, v2\n"
      "ds_write_b128 v63, v[30:33] offset:0\n"
      "s_add_u32 s58, s73, 12288\n"
      "v_add_u32 v63, s58, v2\n"
      "ds_write_b128 v63, v[34:37] offset:0\n"
      "s_add_u32 s58, s73, 13312\n"
      "v_add_u32 v63, s58, v2\n"
      "ds_write_b128 v63, v[38:41] offset:0\n"
      "s_add_u32 s58, s73, 14336\n"
      "v_add_u32 v63, s58, v2\n"
      "ds_write_b128 v63, v[42:45] offset:0\n"
      "s_waitcnt vmcnt(10)\n"
      "s_waitcnt lgkmcnt(0)\n"
      "s_barrier\n"
      "s_mul_i32 s57, s47, 12\n"
      "s_lshr_b32 s57, s57, 3\n"
      "s_add_u32 s57, s57, 3\n"
      "s_sub_u32 s59, s57, 12\n"
      "s_cselect_b32 s57, s57, s59\n"
      "s_lshl_b32 s56, s57, 14\n"
      "s_mul_i32 s59, s50, 704512\n"
      "s_add_u32 s56, s56, s59\n"
      "s_lshl_b32 s57, s57, 11\n"
      "s_add_u32 m0, s57, s62\n"
      "s_nop 0\n"
      "buffer_load_dwordx4 v62, s[20:23], s56 offen lds\n"
      "s_mul_i32 s57, s47, 12\n"
      "s_lshr_b32 s57, s57, 3\n"
      "s_add_u32 s57, s57, 4\n"
      "s_sub_u32 s59, s57, 12\n"
      "s_cselect_b32 s57, s57, s59\n"
      "s_lshl_b32 s56, s57, 14\n"
      "s_mul_i32 s59, s50, 704512\n"
      "s_add_u32 s56, s56, s59\n"
      "s_lshl_b32 s57, s57, 11\n"
      "s_add_u32 m0, s57, s62\n"
      "s_nop 0\n"
      "buffer_load_dwordx4 v62, s[20:23], s56 offen lds\n"
      "s_mul_i32 s57, s47, 12\n"
      "s_lshr_b32 s57, s57, 3\n"
      "s_add_u32 s57, s57, 5\n"
      "s_sub_u32 s59, s57, 12\n"
      "s_cselect_b32 s57, s57, s59\n"
      "s_lshl_b32 s56, s57, 14\n"
      "s_mul_i32 s59, s50, 704512\n"
      "s_add_u32 s56, s56, s59\n"
      "s_lshl_b32 s57, s57, 11\n"
      "s_add_u32 m0, s57, s62\n"
      "s_nop 0\n"
      "buffer_load_dwordx4 v62, s[20:23], s56 offen lds\n"
      "s_mul_i32 s57, s47, 12\n"
      "s_lshr_b32 s57, s57, 3\n"
      "s_add_u32 s57, s57, 6\n"
      "s_sub_u32 s59, s57, 12\n"
      "s_cselect_b32 s57, s57, s59\n"
      "s_lshl_b32 s56, s57, 14\n"
      "s_mul_i32 s59, s50, 704512\n"
      "s_add_u32 s56, s56, s59\n"
      "s_lshl_b32 s57, s57, 11\n"
      "s_add_u32 m0, s57, s62\n"
      "s_nop 0\n"
      "buffer_load_dwordx4 v62, s[20:23], s56 offen lds\n"
      "s_mul_i32 s57, s47, 12\n"
      "s_lshr_b32 s57, s57, 3\n"
      "s_add_u32 s57, s57, 7\n"
      "s_sub_u32 s59, s57, 12\n"
      "s_cselect_b32 s57, s57, s59\n"
      "s_lshl_b32 s56, s57, 14\n"
      "s_mul_i32 s59, s50, 704512\n"
      "s_add_u32 s56, s56, s59\n"
      "s_lshl_b32 s57, s57, 11\n"
      "s_add_u32 m0, s57, s62\n"
      "s_nop 0\n"
      "buffer_load_dwordx4 v62, s[20:23], s56 offen lds\n"
      "s_mul_i32 s57, s47, 12\n"
      "s_lshr_b32 s57, s57, 3\n"
      "s_add_u32 s57, s57, 8\n"
      "s_sub_u32 s59, s57, 12\n"
      "s_cselect_b32 s57, s57, s59\n"
      "s_lshl_b32 s56, s57, 14\n"
      "s_mul_i32 s59, s50, 704512\n"
      "s_add_u32 s56, s56, s59\n"
      "s_lshl_b32 s57, s57, 11\n"
      "s_add_u32 m0, s57, s62\n"
      "s_nop 0\n"
      "buffer_load_dwordx4 v62, s[20:23], s56 offen lds\n"
      "s_barrier\n"
      "s_waitcnt vmcnt(15)\n"
      "s_mul_i32 s57, s47, 12\n"
      "s_lshr_b32 s57, s57, 3\n"
      "s_add_u32 s57, s57, 9\n"
      "s_sub_u32 s59, s57, 12\n"
      "s_cselect_b32 s57, s57, s59\n"
      "s_lshl_b32 s56, s57, 14\n"
      "s_mul_i32 s59, s50, 704512\n"
      "s_add_u32 s56, s56, s59\n"
      "s_lshl_b32 s57, s57, 11\n"
      "s_add_u32 m0, s57, s62\n"
      "s_nop 0\n"
      "buffer_load_dwordx4 v62, s[20:23], s56 offen lds\n"
      "s_mul_i32 s57, s47, 12\n"
      "s_lshr_b32 s57, s57, 3\n"
      "s_add_u32 s57, s57, 0\n"
      "s_sub_u32 s59, s57, 12\n"
      "s_cselect_b32 s57, s57, s59\n"
      "s_lshl_b32 s57, s57, 11\n"
      "s_add_u32 s58, s57, s62\n"
      "v_add_u32 v63, s58, v2\n"
      "s_waitcnt vmcnt(9)\n"
      "ds_write_b128 v63, v[90:93] offset:0\n"
      "s_mul_i32 s57, s47, 12\n"
      "s_lshr_b32 s57, s57, 3\n"
      "s_add_u32 s57, s57, 10\n"
      "s_sub_u32 s59, s57, 12\n"
      "s_cselect_b32 s57, s57, s59\n"
      "s_lshl_b32 s56, s57, 14\n"
      "s_mul_i32 s59, s50, 704512\n"
      "s_add_u32 s56, s56, s59\n"
      "s_lshl_b32 s57, s57, 11\n"
      "s_add_u32 m0, s57, s62\n"
      "s_nop 0\n"
      "buffer_load_dwordx4 v62, s[20:23], s56 offen lds\n"
      "s_mul_i32 s57, s47, 12\n"
      "s_lshr_b32 s57, s57, 3\n"
      "s_add_u32 s57, s57, 1\n"
      "s_sub_u32 s59, s57, 12\n"
      "s_cselect_b32 s57, s57, s59\n"
      "s_lshl_b32 s57, s57, 11\n"
      "s_add_u32 s58, s57, s62\n"
      "v_add_u32 v63, s58, v2\n"
      "s_waitcnt vmcnt(9)\n"
      "ds_write_b128 v63, v[94:97] offset:0\n"
      "s_mul_i32 s57, s47, 12\n"
      "s_lshr_b32 s57, s57, 3\n"
      "s_add_u32 s57, s57, 11\n"
      "s_sub_u32 s59, s57, 12\n"
      "s_cselect_b32 s57, s57, s59\n"
      "s_lshl_b32 s56, s57, 14\n"
      "s_mul_i32 s59, s50, 704512\n"
      "s_add_u32 s56, s56, s59\n"
      "s_lshl_b32 s57, s57, 11\n"
      "s_add_u32 m0, s57, s62\n"
      "s_nop 0\n"
      "buffer_load_dwordx4 v62, s[20:23], s56 offen lds\n"
      "s_mul_i32 s57, s47, 12\n"
      "s_lshr_b32 s57, s57, 3\n"
      "s_add_u32 s57, s57, 2\n"
      "s_sub_u32 s59, s57, 12\n"
      "s_cselect_b32 s57, s57, s59\n"
      "s_lshl_b32 s57, s57, 11\n"
      "s_add_u32 s58, s57, s62\n"
      "v_add_u32 v63, s58, v2\n"
      "s_waitcnt vmcnt(9)\n"
      "ds_write_b128 v63, v[98:101] offset:0\n"
      "s_mul_i32 s57, s47, 24\n"
      "s_lshr_b32 s57, s57, 3\n"
      "s_add_u32 s57, s57, 0\n"
      "s_sub_u32 s59, s57, 24\n"
      "s_cselect_b32 s57, s57, s59\n"
      "s_lshl_b32 s56, s57, 14\n"
      "s_mul_i32 s59, s50, 704512\n"
      "s_add_u32 s56, s56, s59\n"
      ".Lpage2_%=:\n"
      "s_cbranch_execz .Lret_%=\n"
      "s_add_u32 s56, s56, 196608\n"
      "buffer_load_dwordx4 v[22:25], v62, s[20:23], s56 offen\n"
      "s_mul_i32 s57, s47, 24\n"
      "s_lshr_b32 s57, s57, 3\n"
      "s_add_u32 s57, s57, 1\n"
      "s_sub_u32 s59, s57, 24\n"
      "s_cselect_b32 s57, s57, s59\n"
      "s_lshl_b32 s56, s57, 14\n"
      "s_mul_i32 s59, s50, 704512\n"
      "s_add_u32 s56, s56, s59\n"
      "s_add_u32 s56, s56, 196608\n"
      "buffer_load_dwordx4 v[26:29], v62, s[20:23], s56 offen\n"
      "s_mul_i32 s57, s47, 24\n"
      "s_lshr_b32 s57, s57, 3\n"
      "s_add_u32 s57, s57, 2\n"
      "s_sub_u32 s59, s57, 24\n"
      "s_cselect_b32 s57, s57, s59\n"
      "s_lshl_b32 s56, s57, 14\n"
      "s_mul_i32 s59, s50, 704512\n"
      "s_add_u32 s56, s56, s59\n"
      "s_add_u32 s56, s56, 196608\n"
      "buffer_load_dwordx4 v[30:33], v62, s[20:23], s56 offen\n"
      "s_mul_i32 s57, s47, 24\n"
      "s_lshr_b32 s57, s57, 3\n"
      "s_add_u32 s57, s57, 3\n"
      "s_sub_u32 s59, s57, 24\n"
      "s_cselect_b32 s57, s57, s59\n"
      "s_lshl_b32 s56, s57, 14\n"
      "s_mul_i32 s59, s50, 704512\n"
      "s_add_u32 s56, s56, s59\n"
      "s_add_u32 s56, s56, 196608\n"
      "buffer_load_dwordx4 v[34:37], v62, s[20:23], s56 offen\n"
      "s_mul_i32 s57, s47, 24\n"
      "s_lshr_b32 s57, s57, 3\n"
      "s_add_u32 s57, s57, 4\n"
      "s_sub_u32 s59, s57, 24\n"
      "s_cselect_b32 s57, s57, s59\n"
      "s_lshl_b32 s56, s57, 14\n"
      "s_mul_i32 s59, s50, 704512\n"
      "s_add_u32 s56, s56, s59\n"
      "s_add_u32 s56, s56, 196608\n"
      "buffer_load_dwordx4 v[38:41], v62, s[20:23], s56 offen\n"
      "s_mul_i32 s57, s47, 24\n"
      "s_lshr_b32 s57, s57, 3\n"
      "s_add_u32 s57, s57, 5\n"
      "s_sub_u32 s59, s57, 24\n"
      "s_cselect_b32 s57, s57, s59\n"
      "s_lshl_b32 s56, s57, 14\n"
      "s_mul_i32 s59, s50, 704512\n"
      "s_add_u32 s56, s56, s59\n"
      "s_add_u32 s56, s56, 196608\n"
      "buffer_load_dwordx4 v[42:45], v62, s[20:23], s56 offen\n"
      "s_mul_i32 s57, s47, 24\n"
      "s_lshr_b32 s57, s57, 3\n"
      "s_add_u32 s57, s57, 6\n"
      "s_sub_u32 s59, s57, 24\n"
      "s_cselect_b32 s57, s57, s59\n"
      "s_lshl_b32 s56, s57, 14\n"
      "s_mul_i32 s59, s50, 704512\n"
      "s_add_u32 s56, s56, s59\n"
      "s_add_u32 s56, s56, 196608\n"
      "buffer_load_dwordx4 v[90:93], v62, s[20:23], s56 offen\n"
      "v_lshl_or_b32 v14, v68, 16, v68\n"
      "v_mov_b32 v15, 0\n"
      "v_mov_b32 v16, 0\n"
      "v_mov_b32 v17, 0\n"
      "s_lshl_b32 s52, s48, 10\n"
      "s_add_u32 s52, s52, 147456\n"
      "v_lshl_add_u32 v18, v5, 4, s52\n"
      "ds_write_b128 v18, v[14:17] offset:512\n"
      "v_add_u32 v18, s52, v2\n"
      "v_lshlrev_b32 v68, 3, v5\n"
      "v_lshl_add_u32 v68, v6, 11, v68\n"
      "s_lshl_b32 s72, s48, 12\n"
      "s_add_u32 s72, s72, 524288\n"
      "s_and_b32 s59, s72, 0xffffe000\n"
      "s_add_u32 s72, s72, s59\n"
      "s_add_u32 s72, s72, s76\n"
      "s_waitcnt lgkmcnt(0)\n"
      "s_barrier\n"
      "buffer_load_dwordx2 v[46:47], v68, s[24:27], s72 offen sc1\n"
      "buffer_load_dwordx2 v[94:95], v68, s[24:27], s72 offen offset:256 sc1\n"
      "buffer_load_dwordx2 v[96:97], v68, s[24:27], s72 offen offset:512 sc1\n"
      "buffer_load_dwordx2 v[98:99], v68, s[24:27], s72 offen offset:768 sc1\n"
      "buffer_load_dwordx2 v[100:101], v68, s[24:27], s72 offen offset:1024 sc1\n"
      "buffer_load_dwordx2 v[102:103], v68, s[24:27], s72 offen offset:1280 sc1\n"
      "buffer_load_dwordx2 v[104:105], v68, s[24:27], s72 offen offset:1536 sc1\n"
      "buffer_load_dwordx2 v[106:107], v68, s[24:27], s72 offen offset:1792 sc1\n"
      "s_waitcnt vmcnt(6)\n"
      "v_add_f32 v46, v46, v94\n"
      "v_add_f32 v47, v47, v95\n"
      "s_waitcnt vmcnt(5)\n"
      "v_add_f32 v46, v46, v96\n"
      "v_add_f32 v47, v47, v97\n"
      "s_waitcnt vmcnt(4)\n"
      "v_add_f32 v46, v46, v98\n"
      "v_add_f32 v47, v47, v99\n"
      "s_waitcnt vmcnt(3)\n"
      "v_add_f32 v46, v46, v100\n"
      "v_add_f32 v47, v47, v101\n"
      "s_waitcnt vmcnt(2)\n"
      "v_add_f32 v46, v46, v102\n"
      "v_add_f32 v47, v47, v103\n"
      "s_waitcnt vmcnt(1)\n"
      "v_add_f32 v46, v46, v104\n"
      "v_add_f32 v47, v47, v105\n"
      "s_waitcnt vmcnt(0)\n"
      "v_add_f32 v46, v46, v106\n"
      "v_add_f32 v47, v47, v107\n"
      "v_mov_b32 v94, v46\n"
      "s_nop 1\n"
      "v_permlane32_swap_b32 v46, v94\n"
      "v_add_f32 v46, v46, v94\n"
      "v_mov_b32 v95, v47\n"
      "s_nop 1\n"
      "v_permlane32_swap_b32 v47, v95\n"
      "v_add_f32 v47, v47, v95\n"
      "v_mul_f32 v46, 0xbb000000, v46\n"
      "v_mul_f32 v47, 0x3b000000, v47\n"
      "v_fma_f32 v47, -v46, v46, v47\n"
      "v_max_f32 v47, 0, v47\n"
      "v_add_f32 v47, 0x3727c5ac, v47\n"
      "v_sqrt_f32 v94, v47\n"
      "v_cvt_pk_bf16_f32 v96, v46, v46\n"
      "v_lshlrev_b32 v96, 16, v96\n"
      "v_sub_f32 v97, v46, v96\n"
      "v_cvt_pk_bf16_f32 v98, v94, v94\n"
      "v_lshlrev_b32 v98, 16, v98\n"
      "v_sub_f32 v99, v94, v98\n"
      "v_cmp_eq_u32 vcc, 0, v6\n"
      "v_cvt_pk_bf16_f32 v14, v96, v96\n"
      "v_cvt_pk_bf16_f32 v15, v97, 0\n"
      "v_cvt_pk_bf16_f32 v100, v98, v99\n"
      "v_cndmask_b32 v14, v100, v14, vcc\n"
      "v_cndmask_b32 v15, 0, v15, vcc\n"
      "ds_write_b128 v18, v[14:17] offset:4096\n"
      "s_lshl_b32 s52, s48, 7\n"
      "s_add_u32 s52, s52, 155648\n"
      "v_lshl_add_u32 v68, v5, 2, s52\n"
      "ds_write_b32 v68, v47\n"
      "s_waitcnt lgkmcnt(0)\n"
      "s_barrier\n"
      "s_barrier\n"
      "s_mul_i32 s57, s47, 24\n"
      "s_lshr_b32 s57, s57, 3\n"
      "s_add_u32 s57, s57, 7\n"
      "s_sub_u32 s59, s57, 24\n"
      "s_cselect_b32 s57, s57, s59\n"
      "s_lshl_b32 s56, s57, 14\n"
      "s_mul_i32 s59, s50, 704512\n"
      "s_add_u32 s56, s56, s59\n"
      "s_add_u32 s56, s56, 196608\n"
      "s_lshl_b32 s57, s57, 11\n"
      "s_mul_i32 s59, s50, 24576\n"
      "s_add_u32 s57, s57, s59\n"
      "s_add_u32 s57, s57, 49152\n"
      "s_add_u32 m0, s57, s62\n"
      "s_nop 0\n"
      "buffer_load_dwordx4 v62, s[20:23], s56 offen lds\n"
      "s_mul_i32 s57, s47, 24\n"
      "s_lshr_b32 s57, s57, 3\n"
      "s_add_u32 s57, s57, 8\n"
      "s_sub_u32 s59, s57, 24\n"
      "s_cselect_b32 s57, s57, s59\n"
      "s_lshl_b32 s56, s57, 14\n"
      "s_mul_i32 s59, s50, 704512\n"
      "s_add_u32 s56, s56, s59\n"
      "s_add_u32 s56, s56, 196608\n"
      "s_lshl_b32 s57, s57, 11\n"
      "s_mul_i32 s59, s50, 24576\n"
      "s_add_u32 s57, s57, s59\n"
      "s_add_u32 s57, s57, 49152\n"
      "s_add_u32 m0, s57, s62\n"
      "s_nop 0\n"
      "buffer_load_dwordx4 v62, s[20:23], s56 offen lds\n"
      "s_mul_i32 s57, s47, 24\n"
      "s_lshr_b32 s57, s57, 3\n"
      "s_add_u32 s57, s57, 9\n"
      "s_sub_u32 s59, s57, 24\n"
      "s_cselect_b32 s57, s57, s59\n"
      "s_lshl_b32 s56, s57, 14\n"
      "s_mul_i32 s59, s50, 704512\n"
      "s_add_u32 s56, s56, s59\n"
      "s_add_u32 s56, s56, 196608\n"
      "s_lshl_b32 s57, s57, 11\n"
      "s_mul_i32 s59, s50, 24576\n"
      "s_add_u32 s57, s57, s59\n"
      "s_add_u32 s57, s57, 49152\n"
      "s_add_u32 m0, s57, s62\n"
      "s_nop 0\n"
      "buffer_load_dwordx4 v62, s[20:23], s56 offen lds\n"
      "s_mul_i32 s57, s47, 24\n"
      "s_lshr_b32 s57, s57, 3\n"
      "s_add_u32 s57, s57, 10\n"
      "s_sub_u32 s59, s57, 24\n"
      "s_cselect_b32 s57, s57, s59\n"
      "s_lshl_b32 s56, s57, 14\n"
      "s_mul_i32 s59, s50, 704512\n"
      "s_add_u32 s56, s56, s59\n"
      "s_add_u32 s56, s56, 196608\n"
      "s_lshl_b32 s57, s57, 11\n"
      "s_mul_i32 s59, s50, 24576\n"
      "s_add_u32 s57, s57, s59\n"
      "s_add_u32 s57, s57, 49152\n"
      "s_add_u32 m0, s57, s62\n"
      "s_nop 0\n"
      "buffer_load_dwordx4 v62, s[20:23], s56 offen lds\n"
      "s_mul_i32 s57, s47, 24\n"
      "s_lshr_b32 s57, s57, 3\n"
      "s_add_u32 s57, s57, 11\n"
      "s_sub_u32 s59, s57, 24\n"
      "s_cselect_b32 s57, s57, s59\n"
      "s_lshl_b32 s56, s57, 14\n"
      "s_mul_i32 s59, s50, 704512\n"
      "s_add_u32 s56, s56, s59\n"
      "s_add_u32 s56, s56, 196608\n"
      "s_lshl_b32 s57, s57, 11\n"
      "s_mul_i32 s59, s50, 24576\n"
      "s_add_u32 s57, s57, s59\n"
      "s_add_u32 s57, s57, 49152\n"
      "s_add_u32 m0, s57, s62\n"
      "s_nop 0\n"
      "buffer_load_dwordx4 v62, s[20:23], s56 offen lds\n"
      "s_mul_i32 s57, s47, 24\n"
      "s_lshr_b32 s57, s57, 3\n"
      "s_add_u32 s57, s57, 12\n"
      "s_sub_u32 s59, s57, 24\n"
      "s_cselect_b32 s57, s57, s59\n"
      "s_lshl_b32 s56, s57, 14\n"
      "s_mul_i32 s59, s50, 704512\n"
      "s_add_u32 s56, s56, s59\n"
      "s_add_u32 s56, s56, 196608\n"
      "s_lshl_b32 s57, s57, 11\n"
      "s_mul_i32 s59, s50, 24576\n"
      "s_add_u32 s57, s57, s59\n"
      "s_add_u32 s57, s57, 49152\n"
      "s_add_u32 m0, s57, s62\n"
      "s_nop 0\n"
      "buffer_load_dwordx4 v62, s[20:23], s56 offen lds\n"
      "s_barrier\n"
      "s_mul_i32 s57, s47, 24\n"
      "s_lshr_b32 s57, s57, 3\n"
      "s_add_u32 s57, s57, 13\n"
      "s_sub_u32 s59, s57, 24\n"
      "s_cselect_b32 s57, s57, s59\n"
      "s_lshl_b32 s56, s57, 14\n"
      "s_mul_i32 s59, s50, 704512\n"
      "s_add_u32 s56, s56, s59\n"
      "s_add_u32 s56, s56, 196608\n"
      "s_lshl_b32 s57, s57, 11\n"
      "s_mul_i32 s59, s50, 24576\n"
      "s_add_u32 s57, s57, s59\n"
      "s_add_u32 s57, s57, 49152\n"
      "s_add_u32 m0, s57, s62\n"
      "s_nop 0\n"
      "buffer_load_dwordx4 v62, s[20:23], s56 offen lds\n"
      "s_mul_i32 s57, s47, 24\n"
      "s_lshr_b32 s57, s57, 3\n"
      "s_add_u32 s57, s57, 0\n"
      "s_sub_u32 s59, s57, 24\n"
      "s_cselect_b32 s57, s57, s59\n"
      "s_lshl_b32 s57, s57, 11\n"
      "s_mul_i32 s59, s50, 24576\n"
      "s_add_u32 s57, s57, s59\n"
      "s_add_u32 s57, s57, 49152\n"
      "s_add_u32 s58, s57, s62\n"
      "v_add_u32 v63, s58, v2\n"
      "ds_write_b128 v63, v[22:25] offset:0\n"
      "s_mul_i32 s57, s47, 24\n"
      "s_lshr_b32 s57, s57, 3\n"
      "s_add_u32 s57, s57, 14\n"
      "s_sub_u32 s59, s57, 24\n"
      "s_cselect_b32 s57, s57, s59\n"
      "s_lshl_b32 s56, s57, 14\n"
      "s_mul_i32 s59, s50, 704512\n"
      "s_add_u32 s56, s56, s59\n"
      "s_add_u32 s56, s56, 196608\n"
      "s_lshl_b32 s57, s57, 11\n"
      "s_mul_i32 s59, s50, 24576\n"
      "s_add_u32 s57, s57, s59\n"
      "s_add_u32 s57, s57, 49152\n"
      "s_add_u32 m0, s57, s62\n"
      "s_nop 0\n"
      "buffer_load_dwordx4 v62, s[20:23], s56 offen lds\n"
      "s_mul_i32 s57, s47, 24\n"
      "s_lshr_b32 s57, s57, 3\n"
      "s_add_u32 s57, s57, 1\n"
      "s_sub_u32 s59, s57, 24\n"
      "s_cselect_b32 s57, s57, s59\n"
      "s_lshl_b32 s57, s57, 11\n"
      "s_mul_i32 s59, s50, 24576\n"
      "s_add_u32 s57, s57, s59\n"
      "s_add_u32 s57, s57, 49152\n"
      "s_add_u32 s58, s57, s62\n"
      "v_add_u32 v63, s58, v2\n"
      "ds_write_b128 v63, v[26:29] offset:0\n"
      "s_mul_i32 s57, s47, 24\n"
      "s_lshr_b32 s57, s57, 3\n"
      "s_add_u32 s57, s57, 15\n"
      "s_sub_u32 s59, s57, 24\n"
      "s_cselect_b32 s57, s57, s59\n"
      "s_lshl_b32 s56, s57, 14\n"
      "s_mul_i32 s59, s50, 704512\n"
      "s_add_u32 s56, s56, s59\n"
      "s_add_u32 s56, s56, 196608\n"
      "s_lshl_b32 s57, s57, 11\n"
      "s_mul_i32 s59, s50, 24576\n"
      "s_add_u32 s57, s57, s59\n"
      "s_add_u32 s57, s57, 49152\n"
      "s_add_u32 m0, s57, s62\n"
      "s_nop 0\n"
      "buffer_load_dwordx4 v62, s[20:23], s56 offen lds\n"
      "s_mul_i32 s57, s47, 24\n"
      "s_lshr_b32 s57, s57, 3\n"
      "s_add_u32 s57, s57, 16\n"
      "s_sub_u32 s59, s57, 24\n"
      "s_cselect_b32 s57, s57, s59\n"
      "s_lshl_b32 s56, s57, 14\n"
      "s_mul_i32 s59, s50, 704512\n"
      "s_add_u32 s56, s56, s59\n"
      "s_add_u32 s56, s56, 196608\n"
      "s_lshl_b32 s57, s57, 11\n"
      "s_mul_i32 s59, s50, 24576\n"
      "s_add_u32 s57, s57, s59\n"
      "s_add_u32 s57, s57, 49152\n"
      "s_add_u32 m0, s57, s62\n"
      "s_nop 0\n"
      "buffer_load_dwordx4 v62, s[20:23], s56 offen lds\n"
      "s_mul_i32 s57, s47, 24\n"
      "s_lshr_b32 s57, s57, 3\n"
      "s_add_u32 s57, s57, 17\n"
      "s_sub_u32 s59, s57, 24\n"
      "s_cselect_b32 s57, s57, s59\n"
      "s_lshl_b32 s56, s57, 14\n"
      "s_mul_i32 s59, s50, 704512\n"
      "s_add_u32 s56, s56, s59\n"
      "s_add_u32 s56, s56, 196608\n"
      "s_lshl_b32 s57, s57, 11\n"
      "s_mul_i32 s59, s50, 24576\n"
      "s_add_u32 s57, s57, s59\n"
      "s_add_u32 s57, s57, 49152\n"
      "s_add_u32 m0, s57, s62\n"
      "s_nop 0\n"
      "buffer_load_dwordx4 v62, s[20:23], s56 offen lds\n"
      "s_mul_i32 s57, s47, 24\n"
      "s_lshr_b32 s57, s57, 3\n"
      "s_add_u32 s57, s57, 18\n"
      "s_sub_u32 s59, s57, 24\n"
      "s_cselect_b32 s57, s57, s59\n"
      "s_lshl_b32 s56, s57, 14\n"
      "s_mul_i32 s59, s50, 704512\n"
      "s_add_u32 s56, s56, s59\n"
      "s_add_u32 s56, s56, 196608\n"
      "s_lshl_b32 s57, s57, 11\n"
      "s_mul_i32 s59, s50, 24576\n"
      "s_add_u32 s57, s57, s59\n"
      "s_add_u32 s57, s57, 49152\n"
      "s_add_u32 m0, s57, s62\n"
      "s_nop 0\n"
      "buffer_load_dwordx4 v62, s[20:23], s56 offen lds\n"
      "s_mul_i32 s57, s47, 24\n"
      "s_lshr_b32 s57, s57, 3\n"
      "s_add_u32 s57, s57, 19\n"
      "s_sub_u32 s59, s57, 24\n"
      "s_cselect_b32 s57, s57, s59\n"
      "s_lshl_b32 s56, s57, 14\n"
      "s_mul_i32 s59, s50, 704512\n"
      "s_add_u32 s56, s56, s59\n"
      "s_add_u32 s56, s56, 196608\n"
      "s_lshl_b32 s57, s57, 11\n"
      "s_mul_i32 s59, s50, 24576\n"
      "s_add_u32 s57, s57, s59\n"
      "s_add_u32 s57, s57, 49152\n"
      "s_add_u32 m0, s57, s62\n"
      "s_nop 0\n"
      "buffer_load_dwordx4 v62, s[20:23], s56 offen lds\n"
      "s_mul_i32 s57, s47, 24\n"
      "s_lshr_b32 s57, s57, 3\n"
      "s_add_u32 s57, s57, 20\n"
      "s_sub_u32 s59, s57, 24\n"
      "s_cselect_b32 s57, s57, s59\n"
      "s_lshl_b32 s56, s57, 14\n"
      "s_mul_i32 s59, s50, 704512\n"
      "s_add_u32 s56, s56, s59\n"
      "s_add_u32 s56, s56, 196608\n"
      "s_lshl_b32 s57, s57, 11\n"
      "s_mul_i32 s59, s50, 24576\n"
      "s_add_u32 s57, s57, s59\n"
      "s_add_u32 s57, s57, 49152\n"
      "s_add_u32 m0, s57, s62\n"
      "s_nop 0\n"
      "buffer_load_dwordx4 v62, s[20:23], s56 offen lds\n"
      "s_mul_i32 s57, s47, 24\n"
      "s_lshr_b32 s57, s57, 3\n"
      "s_add_u32 s57, s57, 21\n"
      "s_sub_u32 s59, s57, 24\n"
      "s_cselect_b32 s57, s57, s59\n"
      "s_lshl_b32 s56, s57, 14\n"
      "s_mul_i32 s59, s50, 704512\n"
      "s_add_u32 s56, s56, s59\n"
      "s_add_u32 s56, s56, 196608\n"
      "s_lshl_b32 s57, s57, 11\n"
      "s_mul_i32 s59, s50, 24576\n"
      "s_add_u32 s57, s57, s59\n"
      "s_add_u32 s57, s57, 49152\n"
      "s_add_u32 m0, s57, s62\n"
      "s_nop 0\n"
      "buffer_load_dwordx4 v62, s[20:23], s56 offen lds\n"
      "s_mul_i32 s57, s47, 24\n"
      "s_lshr_b32 s57, s57, 3\n"
      "s_add_u32 s57, s57, 22\n"
      "s_sub_u32 s59, s57, 24\n"
      "s_cselect_b32 s57, s57, s59\n"
      "s_lshl_b32 s56, s57, 14\n"
      "s_mul_i32 s59, s50, 704512\n"
      "s_add_u32 s56, s56, s59\n"
      "s_add_u32 s56, s56, 196608\n"
      "s_lshl_b32 s57, s57, 11\n"
      "s_mul_i32 s59, s50, 24576\n"
      "s_add_u32 s57, s57, s59\n"
      "s_add_u32 s57, s57, 49152\n"
      "s_add_u32 m0, s57, s62\n"
      "s_nop 0\n"
      "buffer_load_dwordx4 v62, s[20:23], s56 offen lds\n"
      "s_waitcnt vmcnt(15)\n"
      "s_mul_i32 s57, s47, 24\n"
      "s_lshr_b32 s57, s57, 3\n"
      "s_add_u32 s57, s57, 23\n"
      "s_sub_u32 s59, s57, 24\n"
      "s_cselect_b32 s57, s57, s59\n"
      "s_lshl_b32 s56, s57, 14\n"
      "s_mul_i32 s59, s50, 704512\n"
      "s_add_u32 s56, s56, s59\n"
      "s_add_u32 s56, s56, 196608\n"
      "s_lshl_b32 s57, s57, 11\n"
      "s_mul_i32 s59, s50, 24576\n"
      "s_add_u32 s57, s57, s59\n"
      "s_add_u32 s57, s57, 49152\n"
      "s_add_u32 m0, s57, s62\n"
      "s_nop 0\n"
      "buffer_load_dwordx4 v62, s[20:23], s56 offen lds\n"
      "s_waitcnt lgkmcnt(0)\n"
      "s_barrier\n"
      "s_mul_i32 s57, s47, 24\n"
      "s_lshr_b32 s57, s57, 3\n"
      "s_add_u32 s57, s57, 2\n"
      "s_sub_u32 s59, s57, 24\n"
      "s_cselect_b32 s57, s57, s59\n"
      "s_lshl_b32 s57, s57, 11\n"
      "s_mul_i32 s59, s50, 24576\n"
      "s_add_u32 s57, s57, s59\n"
      "s_add_u32 s57, s57, 49152\n"
      "s_add_u32 s58, s57, s62\n"
      "v_add_u32 v63, s58, v2\n"
      "ds_write_b128 v63, v[30:33] offset:0\n"
      "s_mul_i32 s57, s47, 24\n"
      "s_lshr_b32 s57, s57, 3\n"
      "s_add_u32 s57, s57, 3\n"
      "s_sub_u32 s59, s57, 24\n"
      "s_cselect_b32 s57, s57, s59\n"
      "s_lshl_b32 s57, s57, 11\n"
      "s_mul_i32 s59, s50, 24576\n"
      "s_add_u32 s57, s57, s59\n"
      "s_add_u32 s57, s57, 49152\n"
      "s_add_u32 s58, s57, s62\n"
      "v_add_u32 v63, s58, v2\n"
      "ds_write_b128 v63, v[34:37] offset:0\n"
      "s_mul_i32 s57, s47, 24\n"
      "s_lshr_b32 s57, s57, 3\n"
      "s_add_u32 s57, s57, 4\n"
      "s_sub_u32 s59, s57, 24\n"
      "s_cselect_b32 s57, s57, s59\n"
      "s_lshl_b32 s57, s57, 11\n"
      "s_mul_i32 s59, s50, 24576\n"
      "s_add_u32 s57, s57, s59\n"
      "s_add_u32 s57, s57, 49152\n"
      "s_add_u32 s58, s57, s62\n"
      "v_add_u32 v63, s58, v2\n"
      "ds_write_b128 v63, v[38:41] offset:0\n"
      "s_mul_i32 s57, s47, 24\n"
      "s_lshr_b32 s57, s57, 3\n"
      "s_add_u32 s57, s57, 5\n"
      "s_sub_u32 s59, s57, 24\n"
      "s_cselect_b32 s57, s57, s59\n"
      "s_lshl_b32 s57, s57, 11\n"
      "s_mul_i32 s59, s50, 24576\n"
      "s_add_u32 s57, s57, s59\n"
      "s_add_u32 s57, s57, 49152\n"
      "s_add_u32 s58, s57, s62\n"
      "v_add_u32 v63, s58, v2\n"
      "ds_write_b128 v63, v[42:45] offset:0\n"
      "s_mul_i32 s57, s47, 24\n"
      "s_lshr_b32 s57, s57, 3\n"
      "s_add_u32 s57, s57, 6\n"
      "s_sub_u32 s59, s57, 24\n"
      "s_cselect_b32 s57, s57, s59\n"
      "s_lshl_b32 s57, s57, 11\n"
      "s_mul_i32 s59, s50, 24576\n"
      "s_add_u32 s57, s57, s59\n"
      "s_add_u32 s57, s57, 49152\n"
      "s_add_u32 s58, s57, s62\n"
      "v_add_u32 v63, s58, v2\n"
      "ds_write_b128 v63, v[90:93] offset:0\n"
      "s_waitcnt vmcnt(0)\n"
      "s_waitcnt lgkmcnt(0)\n"
      "s_barrier\n"
      "s_mul_i32 s57, s47, 7\n"
      "s_lshr_b32 s57, s57, 3\n"
      "s_add_u32 s57, s57, 0\n"
      "s_sub_u32 s59, s57, 7\n"
      "s_cselect_b32 s57, s57, s59\n"
      "s_lshl_b32 s57, s57, 11\n"
      "s_add_u32 s58, s57, s62\n"
      "v_add_u32 v63, s58, v2\n"
      "ds_write_b128 v63, v[50:53] offset:0\n"
      "s_mul_i32 s57, s47, 7\n"
      "s_lshr_b32 s57, s57, 3\n"
      "s_add_u32 s57, s57, 1\n"
      "s_sub_u32 s59, s57, 7\n"
      "s_cselect_b32 s57, s57, s59\n"
      "s_lshl_b32 s57, s57, 11\n"
      "s_add_u32 s58, s57, s62\n"
      "v_add_u32 v63, s58, v2\n"
      "ds_write_b128 v63, v[54:57] offset:0\n"
      "s_mul_i32 s57, s47, 7\n"
      "s_lshr_b32 s57, s57, 3\n"
      "s_add_u32 s57, s57, 2\n"
      "s_sub_u32 s59, s57, 7\n"
      "s_cselect_b32 s57, s57, s59\n"
      "s_lshl_b32 s57, s57, 11\n"
      "s_add_u32 s58, s57, s62\n"
      "v_add_u32 v63, s58, v2\n"
      "ds_write_b128 v63, v[70:73] offset:0\n"
      "s_mul_i32 s57, s47, 7\n"
      "s_lshr_b32 s57, s57, 3\n"
      "s_add_u32 s57, s57, 3\n"
      "s_sub_u32 s59, s57, 7\n"
      "s_cselect_b32 s57, s57, s59\n"
      "s_lshl_b32 s57, s57, 11\n"
      "s_add_u32 s58, s57, s62\n"
      "v_add_u32 v63, s58, v2\n"
      "ds_write_b128 v63, v[74:77] offset:0\n"
      "s_mul_i32 s57, s47, 7\n"
      "s_lshr_b32 s57, s57, 3\n"
      "s_add_u32 s57, s57, 4\n"
      "s_sub_u32 s59, s57, 7\n"
      "s_cselect_b32 s57, s57, s59\n"
      "s_lshl_b32 s57, s57, 11\n"
      "s_add_u32 s58, s57, s62\n"
      "v_add_u32 v63, s58, v2\n"
      "ds_write_b128 v63, v[78:81] offset:0\n"
      "s_mul_i32 s57, s47, 7\n"
      "s_lshr_b32 s57, s57, 3\n"
      "s_add_u32 s57, s57, 5\n"
      "s_sub_u32 s59, s57, 7\n"
      "s_cselect_b32 s57, s57, s59\n"
      "s_lshl_b32 s57, s57, 11\n"
      "s_add_u32 s58, s57, s62\n"
      "v_add_u32 v63, s58, v2\n"
      "ds_write_b128 v63, v[82:85] offset:0\n"
      "s_mul_i32 s57, s47, 7\n"
      "s_lshr_b32 s57, s57, 3\n"
      "s_add_u32 s57, s57, 6\n"
      "s_sub_u32 s59, s57, 7\n"
      "s_cselect_b32 s57, s57, s59\n"
      "s_lshl_b32 s57, s57, 11\n"
      "s_add_u32 s58, s57, s62\n"
      "v_add_u32 v63, s58, v2\n"
      "ds_write_b128 v63, v[86:89] offset:0\n"
      "s_waitcnt lgkmcnt(0)\n"
      "s_barrier\n"
      "v_and_b32 v64, 15, v1\n"
      "v_lshlrev_b32 v64, 21, v64\n"
      "v_bfe_u32 v63, v1, 4, 1\n"
      "v_lshl_add_u32 v64, v63, 13, v64\n"
      "s_lshl_b32 s72, s41, 3\n"
      "s_add_u32 s72, s72, s38\n"
      "s_cmp_eq_u32 s72, 7\n"
      "s_cselect_b32 s52, 0, 1073741824\n"
      "v_add_u32 v63, s52, v64\n"
      "s_mov_b32 s52, 1998848\n"
      "buffer_load_dwordx2 v[66:67], v63, s[24:27], s52 offen sc1\n"
      "s_barrier\n"
      "s_cmp_eq_u32 s72, 7\n"
      "s_cbranch_scc0 .Lnoepoch_1011_%=\n"
      "s_mov_b32 s60, 0x100000\n"
      ".Lepoll_1012_%=:\n"
      "s_waitcnt vmcnt(0)\n"
      "v_cmp_eq_u64 vcc, s[36:37], v[66:67]\n"
      "s_nop 1\n"
      "s_lshr_b32 s53, vcc_lo, 16\n"
      "s_or_b32 s53, s53, vcc_lo\n"
      "s_and_b32 s53, s53, 0xffff\n"
      "s_cmp_eq_u32 s53, 0xffff\n"
      "s_cbranch_scc1 .Lepgo_1013_%=\n"
      "buffer_load_dwordx2 v[66:67], v64, s[24:27], s52 offen sc1\n"
      "s_sub_u32 s60, s60, 1\n"
      "s_cbranch_scc0 .Lepoll_1012_%=\n"
      "s_waitcnt vmcnt(0)\n"
      ".Lepgo_1013_%=:\n"
      "v_mov_b32 v66, s36\n"
      "v_mov_b32 v67, s37\n"
      "v_mov_b32 v64, 33554432\n"
      "buffer_store_dwordx2 v[66:67], v64, s[24:27], 0 offen sc1\n"
      ".Lnoepoch_1011_%=:\n"
      "s_endpgm\n"
      ".Lcompute_%=:\n"
      "v_lshrrev_b32 v3, 3, v1\n"
      "s_and_b32 s48, s38, 3\n"
      "s_lshr_b32 s52, s8, 7\n"
      "s_add_u32 s52, s52, s40\n"
      "s_and_b32 s52, s52, 7\n"
      "s_and_b32 s53, s39, 7\n"
      "s_mul_i32 s53, s53, 4\n"
      "s_lshr_b32 s53, s53, 2\n"
      "s_cmp_eq_u32 s52, 3\n"
      "s_cselect_b32 s53, s53, 0\n"
      "s_add_u32 s42, s53, s35\n"
      "s_sub_u32 s52, s42, 8\n"
      "s_cselect_b32 s42, s42, s52\n"
      "s_lshl_b32 s58, s48, 2\n"
      "s_lshr_b32 s58, 0x3333, s58\n"
      "s_and_b32 s58, s58, 15\n"
      "s_min_u32 s58, s58, 8\n"
      "s_lshl_b32 s58, s58, 12\n"
      "s_getpc_b64 s[56:57]\n"
      ".Lit_2001_%=:\n"
      "s_sub_u32 s56, s56, .Lit_2001_%=-.Lpage1_%=+4096\n"
      "s_subb_u32 s57, s57, 0\n"
      "s_add_u32 s56, s56, s58\n"
      "s_addc_u32 s57, s57, 0\n"
      "s_mov_b64 exec, 0\n"
      "s_swappc_b64 s[58:59], s[56:57]\n"
      "s_mov_b64 exec, -1\n"
      "v_and_b32 v12, 1, v4\n"
      "v_lshlrev_b32 v12, 5, v12\n"
      "s_lshl_b32 s52, s48, 3\n"
      "v_add3_u32 v12, v12, s52, v3\n"
      "v_xor_b32 v12, v12, v4\n"
      "v_lshlrev_b32 v12, 4, v12\n"
      "v_lshrrev_b32 v13, 1, v4\n"
      "v_lshl_add_u32 v12, v13, 10, v12\n"
      "v_add_u32 v13, 155648, v12\n"
      "s_sub_u32 s52, s40, s42\n"
      "s_lshl_b32 s52, s52, 12\n"
      "s_and_b32 s52, s52, 0x7fff\n"
      "v_add_u32 v14, s52, v12\n"
      "s_add_u32 s52, s52, 0x1000\n"
      "s_and_b32 s52, s52, 0x7fff\n"
      "v_add_u32 v15, s52, v12\n"
      "s_add_u32 s52, s52, 0x1000\n"
      "s_and_b32 s52, s52, 0x7fff\n"
      "v_add_u32 v48, s52, v12\n"
      "s_add_u32 s52, s52, 0x1000\n"
      "s_and_b32 s52, s52, 0x7fff\n"
      "v_add_u32 v49, s52, v12\n"
      "s_add_u32 s52, s52, 0x1000\n"
      "s_and_b32 s52, s52, 0x7fff\n"
      "v_add_u32 v50, s52, v12\n"
      "s_add_u32 s52, s52, 0x1000\n"
      "s_and_b32 s52, s52, 0x7fff\n"
      "v_add_u32 v51, s52, v12\n"
      "s_add_u32 s52, s52, 0x1000\n"
      "s_and_b32 s52, s52, 0x7fff\n"
      "v_add_u32 v52, s52, v12\n"
      "s_add_u32 s52, s52, 0x1000\n"
      "s_and_b32 s52, s52, 0x7fff\n"
      "v_add_u32 v12, s52, v12\n"
      "s_lshl_b32 s56, s48, 4\n"
      "v_add_u32 v54, s56, v3\n"
      "v_lshlrev_b32 v53, 1, v3\n"
      "v_and_b32 v53, 4, v53\n"
      "v_xor_b32 v53, v53, v4\n"
      "s_lshl_b32 s57, s40, 7\n"
      "v_lshl_add_u32 v55, v53, 4, s57\n"
      "s_movk_i32 s57, 0xc00\n"
      "v_mad_u32_u24 v53, v54, s57, v55\n"
      "v_add_u32 v54, 0x6000, v53\n"
      "s_add_u32 s64, s42, 0\n"
      "s_and_b32 s64, s64, 7\n"
      "s_mul_i32 s64, s64, 0x30000\n"
      "s_add_u32 s65, s42, 1\n"
      "s_and_b32 s65, s65, 7\n"
      "s_mul_i32 s65, s65, 0x30000\n"
      "s_add_u32 s66, s42, 2\n"
      "s_and_b32 s66, s66, 7\n"
      "s_mul_i32 s66, s66, 0x30000\n"
      "s_add_u32 s67, s42, 3\n"
      "s_and_b32 s67, s67, 7\n"
      "s_mul_i32 s67, s67, 0x30000\n"
      "s_add_u32 s68, s42, 4\n"
      "s_and_b32 s68, s68, 7\n"
      "s_mul_i32 s68, s68, 0x30000\n"
      "s_lshl_b32 s77, s48, 12\n"
      "s_add_u32 s77, s77, 65536\n"
      "v_lshlrev_b32 v2, 4, v1\n"
      "v_and_b32 v5, 31, v1\n"
      "v_lshrrev_b32 v6, 5, v1\n"
      "s_and_b32 s49, s38, 1\n"
      "s_lshr_b32 s50, s48, 1\n"
      "s_and_b32 s75, s39, 1\n"
      "s_lshl_b32 s75, s75, 1\n"
      "s_add_u32 s75, s75, s49\n"
      "v_lshlrev_b32 v55, 2, v6\n"
      "v_sub_u32 v55, v5, v55\n"
      "v_mov_b32 v72, 0xf149f2ca\n"
      "v_cmp_le_i32 s[80:81], 0, v55\n"
      "v_cmp_le_i32 s[82:83], 1, v55\n"
      "v_cmp_le_i32 s[84:85], 2, v55\n"
      "v_cmp_le_i32 s[86:87], 3, v55\n"
      "v_cndmask_b32 v56, v72, 0, s[80:81]\n"
      "v_cndmask_b32 v57, v72, 0, s[82:83]\n"
      "v_cndmask_b32 v58, v72, 0, s[84:85]\n"
      "v_cndmask_b32 v59, v72, 0, s[86:87]\n"
      "v_cmp_le_i32 s[80:81], 8, v55\n"
      "v_cmp_le_i32 s[82:83], 9, v55\n"
      "v_cmp_le_i32 s[84:85], 10, v55\n"
      "v_cmp_le_i32 s[86:87], 11, v55\n"
      "v_cndmask_b32 v60, v72, 0, s[80:81]\n"
      "v_cndmask_b32 v61, v72, 0, s[82:83]\n"
      "v_cndmask_b32 v62, v72, 0, s[84:85]\n"
      "v_cndmask_b32 v63, v72, 0, s[86:87]\n"
      "v_cmp_le_i32 s[80:81], 16, v55\n"
      "v_cmp_le_i32 s[82:83], 17, v55\n"
      "v_cmp_le_i32 s[84:85], 18, v55\n"
      "v_cmp_le_i32 s[86:87], 19, v55\n"
      "v_cndmask_b32 v64, v72, 0, s[80:81]\n"
      "v_cndmask_b32 v65, v72, 0, s[82:83]\n"
      "v_cndmask_b32 v66, v72, 0, s[84:85]\n"
      "v_cndmask_b32 v67, v72, 0, s[86:87]\n"
      "v_cmp_le_i32 s[80:81], 24, v55\n"
      "v_cmp_le_i32 s[82:83], 25, v55\n"
      "v_cmp_le_i32 s[84:85], 26, v55\n"
      "v_cmp_le_i32 s[86:87], 27, v55\n"
      "v_cndmask_b32 v68, v72, 0, s[80:81]\n"
      "v_cndmask_b32 v69, v72, 0, s[82:83]\n"
      "v_cndmask_b32 v70, v72, 0, s[84:85]\n"
      "v_cndmask_b32 v71, v72, 0, s[86:87]\n"
      "s_lshl_b32 s52, s50, 12\n"
      "s_lshl_b32 s54, s49, 10\n"
      "s_add_u32 s54, s54, s52\n"
      "s_add_u32 s54, s54, 131072\n"
      "v_lshl_add_u32 v74, v1, 4, s54\n"
      "s_lshl_b32 s53, s49, 4\n"
      "v_xor_b32 v55, s53, v1\n"
      "v_bfe_u32 v75, v1, 4, 1\n"
      "s_add_u32 s52, s52, 133120\n"
      "v_lshl_add_u32 v55, v55, 4, s52\n"
      "s_sub_u32 s52, 148480, s52\n"
      "v_mad_u32_u24 v86, v75, s52, v55\n"
      "v_xor_b32 v77, 1, v75\n"
      "v_lshlrev_b32 v77, 13, v77\n"
      "v_add_u32 v87, v86, v77\n"
      "s_lshl_b32 s52, s48, 10\n"
      "s_add_u32 s52, s52, 155648\n"
      "v_lshl_add_u32 v76, v1, 4, s52\n"
      "s_lshl_b32 s52, s49, 15\n"
      "v_add_u32 v55, 0, v6\n"
      "v_xor_b32 v55, v55, v1\n"
      "v_lshl_add_u32 v55, v55, 4, s52\n"
      "v_add_u32 v72, 2, v6\n"
      "v_xor_b32 v72, v72, v1\n"
      "v_lshl_add_u32 v72, v72, 4, s52\n"
      "v_add_u32 v88, 4, v6\n"
      "v_xor_b32 v88, v88, v1\n"
      "v_lshl_add_u32 v88, v88, 4, s52\n"
      "v_add_u32 v89, 6, v6\n"
      "v_xor_b32 v89, v89, v1\n"
      "v_lshl_add_u32 v89, v89, 4, s52\n"
      "v_and_b32 v90, 3, v1\n"
      "v_bfe_u32 v91, v1, 4, 1\n"
      "v_lshlrev_b32 v90, 3, v90\n"
      "v_lshl_add_u32 v90, v91, 5, v90\n"
      "v_bfe_u32 v91, v1, 2, 2\n"
      "v_lshrrev_b32 v92, 1, v91\n"
      "v_xor_b32 v92, s50, v92\n"
      "v_lshl_add_u32 v92, v92, 6, v90\n"
      "s_nop 0\n"
      ".Lpage3_%=:\n"
      "s_cbranch_execz .Lret_%=\n"
      "v_lshl_add_u32 v92, v91, 7, v92\n"
      "v_lshl_add_u32 v93, v6, 11, v92\n"
      "v_add_u32 v93, 65536, v93\n"
      "v_add_u32 v94, 0x10000, v93\n"
      "s_lshl_b32 s52, s38, 6\n"
      "v_or_b32 v95, s52, v1\n"
      "v_cmp_gt_u32 vcc, 8, v95\n"
      "v_mov_b32 v95, 1073741824\n"
      "s_nop 0\n"
      "v_cndmask_b32 v95, v95, v2, vcc\n"
      "s_lshl_b32 s61, s39, 1\n"
      "s_add_u32 s61, s61, s49\n"
      "s_lshl_b32 s53, s50, 12\n"
      "s_lshl_b32 s54, s49, 13\n"
      "s_add_u32 s53, s53, s54\n"
      "s_add_u32 s54, s53, 131072\n"
      "v_add_u32 v103, s54, v2\n"
      "s_lshr_b32 s53, s53, 1\n"
      "s_add_u32 s53, s53, 155648\n"
      "v_add_u32 v104, s53, v2\n"
      "s_lshl_b32 s54, s50, 2\n"
      "v_xor_b32 v105, s54, v5\n"
      "v_xor_b32 v105, 0, v105\n"
      "v_lshl_add_u32 v105, v105, 4, s53\n"
      "v_lshl_add_u32 v105, v6, 3, v105\n"
      "v_xor_b32 v114, s54, v5\n"
      "v_xor_b32 v114, 33, v114\n"
      "v_lshl_add_u32 v114, v114, 4, s53\n"
      "v_lshl_add_u32 v114, v6, 3, v114\n"
      "v_xor_b32 v115, s54, v5\n"
      "v_xor_b32 v115, 2, v115\n"
      "v_lshl_add_u32 v115, v115, 4, s53\n"
      "v_lshl_add_u32 v115, v6, 3, v115\n"
      "v_xor_b32 v116, s54, v5\n"
      "v_xor_b32 v116, 35, v116\n"
      "v_lshl_add_u32 v116, v116, 4, s53\n"
      "v_lshl_add_u32 v116, v6, 3, v116\n"
      "s_mov_b32 s28, s90\n"
      "s_and_b32 s29, s91, 0xffff\n"
      "s_mov_b32 s30, 2097152\n"
      "s_mov_b32 s31, 0x20000\n"
      "s_lshl_b32 s55, s40, 7\n"
      "s_lshl_b32 s56, s50, 6\n"
      "s_add_u32 s55, s55, s56\n"
      "s_add_u32 s56, s88, s55\n"
      "s_addc_u32 s57, s89, 0\n"
      "s_load_dwordx16 s[80:95], s[56:57], 0x0\n"
      "s_add_u32 s51, s51, 128\n"
      "s_and_b32 s51, s51, 0x3ff\n"
      "buffer_load_dwordx4 v[40:43], v7, s[4:7], s51 offen\n"
      "s_add_u32 s51, s51, 128\n"
      "s_and_b32 s51, s51, 0x3ff\n"
      "buffer_load_dwordx4 v[44:47], v7, s[4:7], s51 offen\n"
      "s_setprio 2\n"
      "v_mov_b32 v117, 0x3f803f80\n"
      "s_waitcnt vmcnt(10)\n"
      "ds_write_b128 v13, v[16:19] offset:0\n"
      "v_dot2_f32_bf16 v118, v16, v117, 0\n"
      "v_dot2_f32_bf16 v120, v16, v16, 0\n"
      "v_dot2_f32_bf16 v119, v17, v117, 0\n"
      "v_dot2_f32_bf16 v121, v17, v17, 0\n"
      "v_dot2_f32_bf16 v118, v18, v117, v118\n"
      "v_dot2_f32_bf16 v120, v18, v18, v120\n"
      "v_dot2_f32_bf16 v119, v19, v117, v119\n"
      "v_dot2_f32_bf16 v121, v19, v19, v121\n"
      "s_waitcnt vmcnt(6)\n"
      "v_dot2_f32_bf16 v118, v20, v117, v118\n"
      "v_dot2_f32_bf16 v120, v20, v20, v120\n"
      "v_dot2_f32_bf16 v119, v21, v117, v119\n"
      "v_dot2_f32_bf16 v121, v21, v21, v121\n"
      "v_dot2_f32_bf16 v118, v22, v117, v118\n"
      "v_dot2_f32_bf16 v120, v22, v22, v120\n"
      "v_dot2_f32_bf16 v119, v23, v117, v119\n"
      "v_dot2_f32_bf16 v121, v23, v23, v121\n"
      "s_waitcnt vmcnt(5)\n"
      "v_dot2_f32_bf16 v118, v24, v117, v118\n"
      "v_dot2_f32_bf16 v120, v24, v24, v120\n"
      "v_dot2_f32_bf16 v119, v25, v117, v119\n"
      "v_dot2_f32_bf16 v121, v25, v25, v121\n"
      "v_dot2_f32_bf16 v118, v26, v117, v118\n"
      "v_dot2_f32_bf16 v120, v26, v26, v120\n"
      "v_dot2_f32_bf16 v119, v27, v117, v119\n"
      "v_dot2_f32_bf16 v121, v27, v27, v121\n"
      "s_waitcnt vmcnt(4)\n"
      "v_dot2_f32_bf16 v118, v28, v117, v118\n"
      "v_dot2_f32_bf16 v120, v28, v28, v120\n"
      "v_dot2_f32_bf16 v119, v29, v117, v119\n"
      "v_dot2_f32_bf16 v121, v29, v29, v121\n"
      "v_dot2_f32_bf16 v118, v30, v117, v118\n"
      "v_dot2_f32_bf16 v120, v30, v30, v120\n"
      "v_dot2_f32_bf16 v119, v31, v117, v119\n"
      "v_dot2_f32_bf16 v121, v31, v31, v121\n"
      "s_waitcnt vmcnt(3)\n"
      "v_dot2_f32_bf16 v118, v32, v117, v118\n"
      "v_dot2_f32_bf16 v120, v32, v32, v120\n"
      "v_dot2_f32_bf16 v119, v33, v117, v119\n"
      "v_dot2_f32_bf16 v121, v33, v33, v121\n"
      "v_dot2_f32_bf16 v118, v34, v117, v118\n"
      "v_dot2_f32_bf16 v120, v34, v34, v120\n"
      "v_dot2_f32_bf16 v119, v35, v117, v119\n"
      "v_dot2_f32_bf16 v121, v35, v35, v121\n"
      "s_waitcnt vmcnt(2)\n"
      "v_dot2_f32_bf16 v118, v36, v117, v118\n"
      "v_dot2_f32_bf16 v120, v36, v36, v120\n"
      "v_dot2_f32_bf16 v119, v37, v117, v119\n"
      "v_dot2_f32_bf16 v121, v37, v37, v121\n"
      "v_dot2_f32_bf16 v118, v38, v117, v118\n"
      "v_dot2_f32_bf16 v120, v38, v38, v120\n"
      "v_dot2_f32_bf16 v119, v39, v117, v119\n"
      "v_dot2_f32_bf16 v121, v39, v39, v121\n"
      "s_waitcnt vmcnt(1)\n"
      "v_dot2_f32_bf16 v118, v40, v117, v118\n"
      "v_dot2_f32_bf16 v120, v40, v40, v120\n"
      "v_dot2_f32_bf16 v119, v41, v117, v119\n"
      "v_dot2_f32_bf16 v121, v41, v41, v121\n"
      "v_dot2_f32_bf16 v118, v42, v117, v118\n"
      "v_dot2_f32_bf16 v120, v42, v42, v120\n"
      "v_dot2_f32_bf16 v119, v43, v117, v119\n"
      "v_dot2_f32_bf16 v121, v43, v43, v121\n"
      "s_waitcnt vmcnt(0)\n"
      "v_dot2_f32_bf16 v118, v44, v117, v118\n"
      "v_dot2_f32_bf16 v120, v44, v44, v120\n"
      "v_dot2_f32_bf16 v119, v45, v117, v119\n"
      "v_dot2_f32_bf16 v121, v45, v45, v121\n"
      "v_dot2_f32_bf16 v118, v46, v117, v118\n"
      "v_dot2_f32_bf16 v120, v46, v46, v120\n"
      "v_dot2_f32_bf16 v119, v47, v117, v119\n"
      "v_dot2_f32_bf16 v121, v47, v47, v121\n"
      "v_lshlrev_b32 v122, 16, v16\n"
      "v_and_b32 v123, 0xffff0000, v16\n"
      "v_lshlrev_b32 v124, 16, v17\n"
      "v_and_b32 v125, 0xffff0000, v17\n"
      "v_lshlrev_b32 v126, 16, v18\n"
      "v_and_b32 v127, 0xffff0000, v18\n"
      "v_lshlrev_b32 v128, 16, v19\n"
      "v_and_b32 v129, 0xffff0000, v19\n"
      "v_add_f32 v118, v118, v119\n"
      "v_add_f32 v120, v120, v121\n"
      "s_nop 0\n"
      "v_add_f32_dpp v118, v118, v118 quad_perm:[1,0,3,2] row_mask:0xf bank_mask:0xf\n"
      "v_add_f32_dpp v120, v120, v120 quad_perm:[1,0,3,2] row_mask:0xf bank_mask:0xf\n"
      "s_nop 0\n"
      "v_add_f32_dpp v118, v118, v118 quad_perm:[2,3,0,1] row_mask:0xf bank_mask:0xf\n"
      "v_add_f32_dpp v120, v120, v120 quad_perm:[2,3,0,1] row_mask:0xf bank_mask:0xf\n"
      "s_nop 0\n"
      "v_add_f32_dpp v118, v118, v118 row_half_mirror row_mask:0xf bank_mask:0xf\n"
      "v_add_f32_dpp v120, v120, v120 row_half_mirror row_mask:0xf bank_mask:0xf\n"
      "v_mul_f32 v118, 0xbb000000, v118\n"
      "v_mul_f32 v120, 0x3b000000, v120\n"
      "v_fma_f32 v120, -v118, v118, v120\n"
      "v_max_f32 v120, 0, v120\n"
      "v_add_f32 v120, 0x3727c5ac, v120\n"
      "v_rsq_f32 v120, v120\n"
      "s_nop 0\n"
      "v_mul_f32 v118, v118, v120\n"
      "v_pk_fma_f32 v[122:123], v[122:123], v[120:121], v[118:119] op_sel_hi:[1,0,0]\n"
      "v_pk_fma_f32 v[124:125], v[124:125], v[120:121], v[118:119] op_sel_hi:[1,0,0]\n"
      "v_pk_fma_f32 v[126:127], v[126:127], v[120:121], v[118:119] op_sel_hi:[1,0,0]\n"
      "v_pk_fma_f32 v[128:129], v[128:129], v[120:121], v[118:119] op_sel_hi:[1,0,0]\n"
      "v_cvt_pk_bf16_f32 v16, v122, v123\n"
      "v_cvt_pk_bf16_f32 v17, v124, v125\n"
      "v_cvt_pk_bf16_f32 v18, v126, v127\n"
      "v_cvt_pk_bf16_f32 v19, v128, v129\n"
      "ds_write_b128 v14, v[16:19] offset:0\n"
      "s_add_u32 m0, s77, 48128\n"
      "s_nop 0\n"
      "buffer_load_dwordx4 v53, s[8:11], s65 offen offset:1024 lds\n"
      "v_lshlrev_b32 v130, 16, v20\n"
      "v_and_b32 v131, 0xffff0000, v20\n"
      "v_lshlrev_b32 v132, 16, v21\n"
      "v_and_b32 v133, 0xffff0000, v21\n"
      "v_lshlrev_b32 v134, 16, v22\n"
      "v_and_b32 v135, 0xffff0000, v22\n"
      "v_lshlrev_b32 v136, 16, v23\n"
      "v_and_b32 v137, 0xffff0000, v23\n"
      "v_pk_fma_f32 v[130:131], v[130:131], v[120:121], v[118:119] op_sel_hi:[1,0,0]\n"
      "v_pk_fma_f32 v[132:133], v[132:133], v[120:121], v[118:119] op_sel_hi:[1,0,0]\n"
      "v_pk_fma_f32 v[134:135], v[134:135], v[120:121], v[118:119] op_sel_hi:[1,0,0]\n"
      "v_pk_fma_f32 v[136:137], v[136:137], v[120:121], v[118:119] op_sel_hi:[1,0,0]\n"
      "v_cvt_pk_bf16_f32 v20, v130, v131\n"
      "v_cvt_pk_bf16_f32 v21, v132, v133\n"
      "v_cvt_pk_bf16_f32 v22, v134, v135\n"
      "v_cvt_pk_bf16_f32 v23, v136, v137\n"
      "ds_write_b128 v15, v[20:23] offset:0\n"
      "buffer_load_dwordx4 v53, s[8:11], s65 offen offset:2048 lds\n"
      "v_lshlrev_b32 v122, 16, v24\n"
      "v_and_b32 v123, 0xffff0000, v24\n"
      "v_lshlrev_b32 v124, 16, v25\n"
      "v_and_b32 v125, 0xffff0000, v25\n"
      "v_lshlrev_b32 v126, 16, v26\n"
      "v_and_b32 v127, 0xffff0000, v26\n"
      "v_lshlrev_b32 v128, 16, v27\n"
      "v_and_b32 v129, 0xffff0000, v27\n"
      "v_pk_fma_f32 v[122:123], v[122:123], v[120:121], v[118:119] op_sel_hi:[1,0,0]\n"
      "v_pk_fma_f32 v[124:125], v[124:125], v[120:121], v[118:119] op_sel_hi:[1,0,0]\n"
      "v_pk_fma_f32 v[126:127], v[126:127], v[120:121], v[118:119] op_sel_hi:[1,0,0]\n"
      "v_pk_fma_f32 v[128:129], v[128:129], v[120:121], v[118:119] op_sel_hi:[1,0,0]\n"
      "v_cvt_pk_bf16_f32 v24, v122, v123\n"
      "v_cvt_pk_bf16_f32 v25, v124, v125\n"
      "v_cvt_pk_bf16_f32 v26, v126, v127\n"
      "v_cvt_pk_bf16_f32 v27, v128, v129\n"
      "ds_write_b128 v48, v[24:27] offset:0\n"
      "s_add_u32 m0, s77, 50176\n"
      "s_nop 0\n"
      "buffer_load_dwordx4 v54, s[8:11], s65 offen offset:1024 lds\n"
      "v_lshlrev_b32 v130, 16, v28\n"
      "v_and_b32 v131, 0xffff0000, v28\n"
      "v_lshlrev_b32 v132, 16, v29\n"
      "v_and_b32 v133, 0xffff0000, v29\n"
      "v_lshlrev_b32 v134, 16, v30\n"
      "v_and_b32 v135, 0xffff0000, v30\n"
      "v_lshlrev_b32 v136, 16, v31\n"
      "v_and_b32 v137, 0xffff0000, v31\n"
      "v_pk_fma_f32 v[130:131], v[130:131], v[120:121], v[118:119] op_sel_hi:[1,0,0]\n"
      "v_pk_fma_f32 v[132:133], v[132:133], v[120:121], v[118:119] op_sel_hi:[1,0,0]\n"
      "v_pk_fma_f32 v[134:135], v[134:135], v[120:121], v[118:119] op_sel_hi:[1,0,0]\n"
      "v_pk_fma_f32 v[136:137], v[136:137], v[120:121], v[118:119] op_sel_hi:[1,0,0]\n"
      "v_cvt_pk_bf16_f32 v28, v130, v131\n"
      "v_cvt_pk_bf16_f32 v29, v132, v133\n"
      "v_cvt_pk_bf16_f32 v30, v134, v135\n"
      "v_cvt_pk_bf16_f32 v31, v136, v137\n"
      "ds_write_b128 v49, v[28:31] offset:0\n"
      "buffer_load_dwordx4 v54, s[8:11], s65 offen offset:2048 lds\n"
      "v_lshlrev_b32 v122, 16, v32\n"
      "v_and_b32 v123, 0xffff0000, v32\n"
      "v_lshlrev_b32 v124, 16, v33\n"
      "v_and_b32 v125, 0xffff0000, v33\n"
      "v_lshlrev_b32 v126, 16, v34\n"
      "v_and_b32 v127, 0xffff0000, v34\n"
      "v_lshlrev_b32 v128, 16, v35\n"
      "v_and_b32 v129, 0xffff0000, v35\n"
      "v_pk_fma_f32 v[122:123], v[122:123], v[120:121], v[118:119] op_sel_hi:[1,0,0]\n"
      "v_pk_fma_f32 v[124:125], v[124:125], v[120:121], v[118:119] op_sel_hi:[1,0,0]\n"
      "v_pk_fma_f32 v[126:127], v[126:127], v[120:121], v[118:119] op_sel_hi:[1,0,0]\n"
      "v_pk_fma_f32 v[128:129], v[128:129], v[120:121], v[118:119] op_sel_hi:[1,0,0]\n"
      "v_cvt_pk_bf16_f32 v32, v122, v123\n"
      "v_cvt_pk_bf16_f32 v33, v124, v125\n"
      "v_cvt_pk_bf16_f32 v34, v126, v127\n"
      "v_cvt_pk_bf16_f32 v35, v128, v129\n"
      "ds_write_b128 v50, v[32:35] offset:0\n"
      "s_add_u32 m0, s77, 64512\n"
      "s_nop 0\n"
      "buffer_load_dwordx4 v53, s[8:11], s66 offen offset:1024 lds\n"
      "v_lshlrev_b32 v130, 16, v36\n"
      "v_and_b32 v131, 0xffff0000, v36\n"
      "v_lshlrev_b32 v132, 16, v37\n"
      "v_and_b32 v133, 0xffff0000, v37\n"
      "v_lshlrev_b32 v134, 16, v38\n"
      "v_and_b32 v135, 0xffff0000, v38\n"
      "v_lshlrev_b32 v136, 16, v39\n"
      "v_and_b32 v137, 0xffff0000, v39\n"
      "v_pk_fma_f32 v[130:131], v[130:131], v[120:121], v[118:119] op_sel_hi:[1,0,0]\n"
      "v_pk_fma_f32 v[132:133], v[132:133], v[120:121], v[118:119] op_sel_hi:[1,0,0]\n"
      "v_pk_fma_f32 v[134:135], v[134:135], v[120:121], v[118:119] op_sel_hi:[1,0,0]\n"
      "v_pk_fma_f32 v[136:137], v[136:137], v[120:121], v[118:119] op_sel_hi:[1,0,0]\n"
      "v_cvt_pk_bf16_f32 v36, v130, v131\n"
      "v_cvt_pk_bf16_f32 v37, v132, v133\n"
      "v_cvt_pk_bf16_f32 v38, v134, v135\n"
      "v_cvt_pk_bf16_f32 v39, v136, v137\n"
      "ds_write_b128 v51, v[36:39] offset:0\n"
      "buffer_load_dwordx4 v53, s[8:11], s66 offen offset:2048 lds\n"
      "v_lshlrev_b32 v122, 16, v40\n"
      "v_and_b32 v123, 0xffff0000, v40\n"
      "v_lshlrev_b32 v124, 16, v41\n"
      "v_and_b32 v125, 0xffff0000, v41\n"
      "v_lshlrev_b32 v126, 16, v42\n"
      "v_and_b32 v127, 0xffff0000, v42\n"
      "v_lshlrev_b32 v128, 16, v43\n"
      "v_and_b32 v129, 0xffff0000, v43\n"
      "v_pk_fma_f32 v[122:123], v[122:123], v[120:121], v[118:119] op_sel_hi:[1,0,0]\n"
      "v_pk_fma_f32 v[124:125], v[124:125], v[120:121], v[118:119] op_sel_hi:[1,0,0]\n"
      "v_pk_fma_f32 v[126:127], v[126:127], v[120:121], v[118:119] op_sel_hi:[1,0,0]\n"
      "v_pk_fma_f32 v[128:129], v[128:129], v[120:121], v[118:119] op_sel_hi:[1,0,0]\n"
      "v_cvt_pk_bf16_f32 v40, v122, v123\n"
      "v_cvt_pk_bf16_f32 v41, v124, v125\n"
      "v_cvt_pk_bf16_f32 v42, v126, v127\n"
      "v_cvt_pk_bf16_f32 v43, v128, v129\n"
      "ds_write_b128 v52, v[40:43] offset:0\n"
      "s_add_u32 m0, s77, 66560\n"
      "s_nop 0\n"
      "buffer_load_dwordx4 v54, s[8:11], s66 offen offset:1024 lds\n"
      "v_lshlrev_b32 v130, 16, v44\n"
      "v_and_b32 v131, 0xffff0000, v44\n"
      "v_lshlrev_b32 v132, 16, v45\n"
      "v_and_b32 v133, 0xffff0000, v45\n"
      "v_lshlrev_b32 v134, 16, v46\n"
      "v_and_b32 v135, 0xffff0000, v46\n"
      "v_lshlrev_b32 v136, 16, v47\n"
      "v_and_b32 v137, 0xffff0000, v47\n"
      "v_pk_fma_f32 v[130:131], v[130:131], v[120:121], v[118:119] op_sel_hi:[1,0,0]\n"
      "v_pk_fma_f32 v[132:133], v[132:133], v[120:121], v[118:119] op_sel_hi:[1,0,0]\n"
      "v_pk_fma_f32 v[134:135], v[134:135], v[120:121], v[118:119] op_sel_hi:[1,0,0]\n"
      "v_pk_fma_f32 v[136:137], v[136:137], v[120:121], v[118:119] op_sel_hi:[1,0,0]\n"
      "v_cvt_pk_bf16_f32 v44, v130, v131\n"
      "v_cvt_pk_bf16_f32 v45, v132, v133\n"
      "v_cvt_pk_bf16_f32 v46, v134, v135\n"
      "v_cvt_pk_bf16_f32 v47, v136, v137\n"
      "ds_write_b128 v12, v[44:47] offset:0\n"
      "buffer_load_dwordx4 v54, s[8:11], s66 offen offset:2048 lds\n"
      "s_add_u32 m0, s77, -1024\n"
      "s_nop 0\n"
      "buffer_load_dwordx4 v53, s[8:11], s67 offen offset:1024 lds\n"
      "buffer_load_dwordx4 v53, s[8:11], s67 offen offset:2048 lds\n"
      "s_add_u32 m0, s77, 1024\n"
      "s_nop 0\n"
      "buffer_load_dwordx4 v54, s[8:11], s67 offen offset:1024 lds\n"
      "buffer_load_dwordx4 v54, s[8:11], s67 offen offset:2048 lds\n"
      "s_add_u32 m0, s77, 15360\n"
      "s_nop 0\n"
      "buffer_load_dwordx4 v53, s[8:11], s68 offen offset:1024 lds\n"
      "buffer_load_dwordx4 v53, s[8:11], s68 offen offset:2048 lds\n"
      "s_add_u32 m0, s77, 17408\n"
      "s_nop 0\n"
      "buffer_load_dwordx4 v54, s[8:11], s68 offen offset:1024 lds\n"
      "buffer_load_dwordx4 v54, s[8:11], s68 offen offset:2048 lds\n"
      "s_setprio 0\n"
      "v_lshrrev_b32 v101, 2, v1\n"
      "v_and_b32 v101, 7, v101\n"
      "v_lshlrev_b32 v101, 10, v101\n"
      "v_and_b32 v100, 48, v2\n"
      "s_cmp_eq_u32 s38, 0\n"
      "s_cselect_b32 s59, 0, 1073741824\n"
      "v_add3_u32 v101, v101, v100, s59\n"
      "s_and_b32 s72, s39, 1\n"
      "s_lshl_b32 s72, s72, 14\n"
      "s_lshl_b32 s59, s40, 7\n"
      "s_add_u32 s72, s72, s59\n"
      "s_add_u32 s72, s72, 1900544\n"
      "v_lshrrev_b32 v102, 2, v1\n"
      "v_lshlrev_b32 v102, 11, v102\n"
      "v_and_b32 v100, 0xffffe000, v102\n"
      "v_add_u32 v102, v102, v100\n"
      "v_and_b32 v100, 48, v2\n"
      "s_cmp_eq_u32 s38, 0\n"
      "s_cselect_b32 s59, 0, 1073741824\n"
      "v_add3_u32 v102, v102, v100, s59\n"
      "s_lshl_b32 s51, s44, 7\n"
      "s_add_u32 s51, s51, 1933312\n"
      "v_xor_b32 v12, 0xf149f2ca, v56\n"
      "v_xor_b32 v13, 0xf149f2ca, v57\n"
      "v_xor_b32 v14, 0xf149f2ca, v58\n"
      "v_xor_b32 v15, 0xf149f2ca, v59\n"
      "v_xor_b32 v16, 0xf149f2ca, v60\n"
      "v_xor_b32 v17, 0xf149f2ca, v61\n"
      "v_xor_b32 v18, 0xf149f2ca, v62\n"
      "v_xor_b32 v19, 0xf149f2ca, v63\n"
      "v_xor_b32 v20, 0xf149f2ca, v64\n"
      "v_xor_b32 v21, 0xf149f2ca, v65\n"
      "v_xor_b32 v22, 0xf149f2ca, v66\n"
      "v_xor_b32 v23, 0xf149f2ca, v67\n"
      "v_xor_b32 v24, 0xf149f2ca, v68\n"
      "v_xor_b32 v25, 0xf149f2ca, v69\n"
      "v_xor_b32 v26, 0xf149f2ca, v70\n"
      "v_xor_b32 v27, 0xf149f2ca, v71\n"
      "v_subrev_u32 v73, 0x10000, v74\n"
      "v_sub_u32 v85, v86, v77\n"
      "s_cmp_eq_u32 s39, 0\n"
      "s_cselect_b32 s56, 2048, 0\n"
      "v_mad_u32_u24 v85, v75, s56, v85\n"
      "v_sub_u32 v84, v85, v77\n"
      "v_sub_u32 v83, v84, v77\n"
      "s_cmp_eq_u32 s39, 1\n"
      "s_cselect_b32 s56, 2048, 0\n"
      "v_mad_u32_u24 v83, v75, s56, v83\n"
      "v_sub_u32 v82, v83, v77\n"
      "v_sub_u32 v81, v82, v77\n"
      "s_cmp_eq_u32 s39, 2\n"
      "s_cselect_b32 s56, 2048, 0\n"
      "v_mad_u32_u24 v81, v75, s56, v81\n"
      "v_sub_u32 v80, v81, v77\n"
      "v_sub_u32 v79, v80, v77\n"
      "s_cmp_eq_u32 s39, 3\n"
      "s_cselect_b32 s56, 2048, 0\n"
      "v_mad_u32_u24 v79, v75, s56, v79\n"
      "v_sub_u32 v78, v79, v77\n"
      "s_mul_i32 s52, s44, 3\n"
      "s_lshl_b32 s53, s44, 1\n"
      "s_add_u32 s53, s53, 11\n"
      "s_cmp_lt_u32 s44, 11\n"
      "s_cselect_b32 s45, s52, s53\n"
      "s_cselect_b32 s46, 3, 2\n"
      "s_mov_b32 s33, 0x21800000\n"
      "s_mov_b32 s34, 0x71800000\n"
      "s_and_b32 s69, s39, 1\n"
      "s_lshl_b32 s69, s69, 16\n"
      "s_lshl_b32 s59, s40, 12\n"
      "s_add_u32 s69, s69, s59\n"
      "s_lshl_b32 s59, s48, 10\n"
      "s_add_u32 s69, s69, s59\n"
      "s_add_u32 s69, s69, 262144\n"
      "s_and_b32 s59, s69, 0xffffe000\n"
      "s_add_u32 s69, s69, s59\n"
      "s_lshl_b32 s62, s75, 16\n"
      "s_lshl_b32 s59, s40, 13\n"
      "s_add_u32 s62, s62, s59\n"
      "s_lshl_b32 s59, s50, 12\n"
      "s_add_u32 s62, s62, s59\n"
      "s_and_b32 s59, s62, 0xffffe000\n"
      "s_add_u32 s62, s62, s59\n"
      "s_lshl_b32 s79, s44, 7\n"
      "s_add_u32 s79, s79, 1081344\n"
      "v_mov_b32 v7, 0\n"
      "v_readfirstlane_b32 s52, v10\n"
      "v_readfirstlane_b32 s53, v11\n"
      "s_nop 0\n"
      "s_mul_i32 s36, s52, 0x4c957f2d\n"
      "s_mul_hi_u32 s37, s52, 0x4c957f2d\n"
      "s_mul_i32 s52, s52, 0x5851f42d\n"
      "s_mul_i32 s53, s53, 0x4c957f2d\n"
      "s_add_u32 s52, s52, s53\n"
      "s_add_u32 s37, s37, s52\n"
      "s_add_u32 s36, s36, 0xf767814f\n"
      "s_addc_u32 s37, s37, 0x14057b7e\n"
      "v_mov_b32 v96, s36\n"
      "v_mov_b32 v97, s37\n"
      "v_mov_b32 v98, s36\n"
      "v_mov_b32 v99, s37\n"
      "v_not_b32 v28, v96\n"
      "v_not_b32 v29, v97\n"
      "v_not_b32 v30, v98\n"
      "v_not_b32 v31, v99\n"
      "s_lshl_b32 s58, s48, 2\n"
      "s_lshr_b32 s58, 0x2514, s58\n"
      "s_and_b32 s58, s58, 15\n"
      "s_min_u32 s58, s58, 8\n"
      "s_lshl_b32 s58, s58, 12\n"
      "s_getpc_b64 s[56:57]\n"
      ".Lit_2002_%=:\n"
      "s_sub_u32 s56, s56, .Lit_2002_%=-.Lpage1_%=+4096\n"
      "s_subb_u32 s57, s57, 0\n"
      "s_add_u32 s56, s56, s58\n"
      "s_addc_u32 s57, s57, 0\n"
      "s_mov_b64 exec, 0\n"
      "s_swappc_b64 s[58:59], s[56:57]\n"
      "s_mov_b64 exec, -1\n"
      "s_waitcnt lgkmcnt(0)\n"
      "s_barrier\n"
      "ds_read_b128 v[48:51], v55 offset:0\n"
      "ds_read_b64_tr_b16 v[170:171], v93 offset:32768\n"
      "ds_read_b64_tr_b16 v[172:173], v93 offset:33280\n"
      "ds_read_b64_tr_b16 v[194:195], v93 offset:33792\n"
      "ds_read_b64_tr_b16 v[196:197], v93 offset:34304\n"
      "ds_read_b128 v[150:153], v72 offset:1024\n"
      "ds_read_b64_tr_b16 v[174:175], v93 offset:36864\n"
      "ds_read_b64_tr_b16 v[176:177], v93 offset:37376\n"
      "ds_read_b64_tr_b16 v[198:199], v93 offset:37888\n"
      "ds_read_b64_tr_b16 v[200:201], v93 offset:38400\n"
      "s_waitcnt lgkmcnt(7)\n"
      "v_mfma_f32_32x32x16_bf16 v[32:47], v[170:173], v[48:51], 0\n"
      "ds_read_b128 v[154:157], v88 offset:2048\n"
      "ds_read_b64_tr_b16 v[178:179], v93 offset:40960\n"
      "ds_read_b64_tr_b16 v[180:181], v93 offset:41472\n"
      "s_waitcnt lgkmcnt(8)\n"
      "v_mfma_f32_32x32x16_bf16 v[118:133], v[48:51], v[194:197], 0\n"
      "ds_read_b64_tr_b16 v[202:203], v93 offset:41984\n"
      "ds_read_b64_tr_b16 v[204:205], v93 offset:42496\n"
      "s_waitcnt lgkmcnt(7)\n"
      "v_mfma_f32_32x32x16_bf16 v[32:47], v[174:177], v[150:153], v[32:47]\n"
      "ds_read_b128 v[48:51], v89 offset:3072\n"
      "ds_read_b64_tr_b16 v[170:171], v93 offset:45056\n"
      "ds_read_b64_tr_b16 v[172:173], v93 offset:45568\n"
      "s_waitcnt lgkmcnt(8)\n"
      "v_mfma_f32_32x32x16_bf16 v[118:133], v[150:153], v[198:201], v[118:133]\n"
      "ds_read_b64_tr_b16 v[194:195], v93 offset:46080\n"
      "ds_read_b64_tr_b16 v[196:197], v93 offset:46592\n"
      "s_waitcnt vmcnt(12)\n"
      "s_lshl_b32 s63, s43, 7\n"
      "s_add_u32 s63, s63, 33558528\n"
      "buffer_load_dwordx2 v[8:9], v7, s[24:27], s63 offen sc1\n"
      "ds_read_b64 v[106:107], v105 offset:0\n"
      "ds_read_b64 v[108:109], v114 offset:0\n"
      "ds_read_b64 v[110:111], v115 offset:1024\n"
      "ds_read_b64 v[112:113], v116 offset:1024\n"
      "s_barrier\n"
      "s_waitcnt lgkmcnt(11)\n"
      "v_mfma_f32_32x32x16_bf16 v[32:47], v[178:181], v[154:157], v[32:47]\n"
      "ds_read_b128 v[150:153], v55 offset:4096\n"
      "ds_read_b64_tr_b16 v[174:175], v93 offset:49152\n"
      "ds_read_b64_tr_b16 v[176:177], v93 offset:49664\n"
      "s_waitcnt lgkmcnt(12)\n"
      "v_mfma_f32_32x32x16_bf16 v[118:133], v[154:157], v[202:205], v[118:133]\n"
      "ds_read_b64_tr_b16 v[198:199], v93 offset:50176\n"
      "ds_read_b64_tr_b16 v[200:201], v93 offset:50688\n"
      "s_waitcnt lgkmcnt(11)\n"
      "v_mfma_f32_32x32x16_bf16 v[32:47], v[170:173], v[48:51], v[32:47]\n"
      "ds_read_b128 v[154:157], v72 offset:5120\n"
      "ds_read_b64_tr_b16 v[178:179], v93 offset:53248\n"
      "ds_read_b64_tr_b16 v[180:181], v93 offset:53760\n"
      "s_waitcnt lgkmcnt(12)\n"
      "v_mfma_f32_32x32x16_bf16 v[118:133], v[48:51], v[194:197], v[118:133]\n"
      "ds_read_b64_tr_b16 v[202:203], v93 offset:54272\n"
      "ds_read_b64_tr_b16 v[204:205], v93 offset:54784\n"
      "s_waitcnt lgkmcnt(7)\n"
      "v_mfma_f32_32x32x16_bf16 v[32:47], v[174:177], v[150:153], v[32:47]\n"
      "ds_read_b128 v[48:51], v88 offset:6144\n"
      "ds_read_b64_tr_b16 v[170:171], v93 offset:57344\n"
      "ds_read_b64_tr_b16 v[172:173], v93 offset:57856\n"
      "s_waitcnt lgkmcnt(8)\n"
      "v_mfma_f32_32x32x16_bf16 v[118:133], v[150:153], v[198:201], v[118:133]\n"
      "ds_read_b64_tr_b16 v[194:195], v93 offset:58368\n"
      "ds_read_b64_tr_b16 v[196:197], v93 offset:58880\n"
      "s_waitcnt lgkmcnt(7)\n"
      "v_mfma_f32_32x32x16_bf16 v[32:47], v[178:181], v[154:157], v[32:47]\n"
      "ds_read_b128 v[150:153], v89 offset:7168\n"
      "ds_read_b64_tr_b16 v[174:175], v93 offset:61440\n"
      "ds_read_b64_tr_b16 v[176:177], v93 offset:61952\n"
      "s_waitcnt lgkmcnt(8)\n"
      "v_mfma_f32_32x32x16_bf16 v[118:133], v[154:157], v[202:205], v[118:133]\n"
      "ds_read_b64_tr_b16 v[198:199], v93 offset:62464\n"
      "ds_read_b64_tr_b16 v[200:201], v93 offset:62976\n"
      "s_waitcnt vmcnt(9)\n"
      "s_barrier\n"
      "s_waitcnt lgkmcnt(7)\n"
      "v_mfma_f32_32x32x16_bf16 v[32:47], v[170:173], v[48:51], v[32:47]\n"
      "ds_read_b128 v[154:157], v55 offset:8192\n"
      "ds_read_b64_tr_b16 v[178:179], v94 offset:0\n"
      "ds_read_b64_tr_b16 v[180:181], v94 offset:512\n"
      "s_waitcnt lgkmcnt(8)\n"
      "v_mfma_f32_32x32x16_bf16 v[118:133], v[48:51], v[194:197], v[118:133]\n"
      "ds_read_b64_tr_b16 v[202:203], v94 offset:1024\n"
      "ds_read_b64_tr_b16 v[204:205], v94 offset:1536\n"
      "s_waitcnt lgkmcnt(7)\n"
      "v_mfma_f32_32x32x16_bf16 v[32:47], v[174:177], v[150:153], v[32:47]\n"
      "ds_read_b128 v[48:51], v72 offset:9216\n"
      "ds_read_b64_tr_b16 v[170:171], v94 offset:4096\n"
      "ds_read_b64_tr_b16 v[172:173], v94 offset:4608\n"
      "s_waitcnt lgkmcnt(8)\n"
      "v_mfma_f32_32x32x16_bf16 v[118:133], v[150:153], v[198:201], v[118:133]\n"
      "ds_read_b64_tr_b16 v[194:195], v94 offset:5120\n"
      "ds_read_b64_tr_b16 v[196:197], v94 offset:5632\n"
      "s_waitcnt lgkmcnt(7)\n"
      "v_mfma_f32_32x32x16_bf16 v[32:47], v[178:181], v[154:157], v[32:47]\n"
      "ds_read_b128 v[150:153], v88 offset:10240\n"
      "ds_read_b64_tr_b16 v[174:175], v94 offset:8192\n"
      "ds_read_b64_tr_b16 v[176:177], v94 offset:8704\n"
      "s_waitcnt lgkmcnt(8)\n"
      "v_mfma_f32_32x32x16_bf16 v[118:133], v[154:157], v[202:205], v[118:133]\n"
      "ds_read_b64_tr_b16 v[198:199], v94 offset:9216\n"
      "ds_read_b64_tr_b16 v[200:201], v94 offset:9728\n"
      "s_waitcnt lgkmcnt(7)\n"
      "v_mfma_f32_32x32x16_bf16 v[32:47], v[170:173], v[48:51], v[32:47]\n"
      "ds_read_b128 v[154:157], v89 offset:11264\n"
      "ds_read_b64_tr_b16 v[178:179], v94 offset:12288\n"
      "ds_read_b64_tr_b16 v[180:181], v94 offset:12800\n"
      "s_waitcnt lgkmcnt(8)\n"
      "v_mfma_f32_32x32x16_bf16 v[118:133], v[48:51], v[194:197], v[118:133]\n"
      "ds_read_b64_tr_b16 v[202:203], v94 offset:13312\n"
      "ds_read_b64_tr_b16 v[204:205], v94 offset:13824\n"
      "s_waitcnt vmcnt(5)\n"
      "s_barrier\n"
      "s_waitcnt lgkmcnt(7)\n"
      "v_mfma_f32_32x32x16_bf16 v[32:47], v[174:177], v[150:153], v[32:47]\n"
      "ds_read_b128 v[48:51], v55 offset:12288\n"
      "ds_read_b64_tr_b16 v[170:171], v93 offset:0\n"
      "ds_read_b64_tr_b16 v[172:173], v93 offset:512\n"
      "s_waitcnt lgkmcnt(8)\n"
      "v_mfma_f32_32x32x16_bf16 v[118:133], v[150:153], v[198:201], v[118:133]\n"
      "ds_read_b64_tr_b16 v[194:195], v93 offset:1024\n"
      "ds_read_b64_tr_b16 v[196:197], v93 offset:1536\n"
      "s_waitcnt lgkmcnt(7)\n"
      "v_mfma_f32_32x32x16_bf16 v[32:47], v[178:181], v[154:157], v[32:47]\n"
      "ds_read_b128 v[150:153], v72 offset:13312\n"
      "ds_read_b64_tr_b16 v[174:175], v93 offset:4096\n"
      "ds_read_b64_tr_b16 v[176:177], v93 offset:4608\n"
      "s_waitcnt lgkmcnt(8)\n"
      "v_mfma_f32_32x32x16_bf16 v[118:133], v[154:157], v[202:205], v[118:133]\n"
      "ds_read_b64_tr_b16 v[198:199], v93 offset:5120\n"
      "ds_read_b64_tr_b16 v[200:201], v93 offset:5632\n"
      "s_waitcnt lgkmcnt(7)\n"
      "v_mfma_f32_32x32x16_bf16 v[32:47], v[170:173], v[48:51], v[32:47]\n"
      "ds_read_b128 v[154:157], v88 offset:14336\n"
      "ds_read_b64_tr_b16 v[178:179], v93 offset:8192\n"
      "ds_read_b64_tr_b16 v[180:181], v93 offset:8704\n"
      "s_waitcnt lgkmcnt(8)\n"
      "v_mfma_f32_32x32x16_bf16 v[118:133], v[48:51], v[194:197], v[118:133]\n"
      "ds_read_b64_tr_b16 v[202:203], v93 offset:9216\n"
      "ds_read_b64_tr_b16 v[204:205], v93 offset:9728\n"
      "s_waitcnt lgkmcnt(7)\n"
      "v_mfma_f32_32x32x16_bf16 v[32:47], v[174:177], v[150:153], v[32:47]\n"
      "ds_read_b128 v[48:51], v89 offset:15360\n"
      "ds_read_b64_tr_b16 v[170:171], v93 offset:12288\n"
      "ds_read_b64_tr_b16 v[172:173], v93 offset:12800\n"
      "s_waitcnt lgkmcnt(8)\n"
      "v_mfma_f32_32x32x16_bf16 v[118:133], v[150:153], v[198:201], v[118:133]\n"
      "ds_read_b64_tr_b16 v[194:195], v93 offset:13312\n"
      "ds_read_b64_tr_b16 v[196:197], v93 offset:13824\n"
      "s_waitcnt vmcnt(1)\n"
      "s_barrier\n"
      "s_waitcnt lgkmcnt(7)\n"
      "v_mfma_f32_32x32x16_bf16 v[32:47], v[178:181], v[154:157], v[32:47]\n"
      "ds_read_b128 v[150:153], v55 offset:16384\n"
      "ds_read_b64_tr_b16 v[174:175], v93 offset:16384\n"
      "ds_read_b64_tr_b16 v[176:177], v93 offset:16896\n"
      "s_waitcnt lgkmcnt(8)\n"
      "v_mfma_f32_32x32x16_bf16 v[118:133], v[154:157], v[202:205], v[118:133]\n"
      "ds_read_b64_tr_b16 v[198:199], v93 offset:17408\n"
      "ds_read_b64_tr_b16 v[200:201], v93 offset:17920\n"
      "s_waitcnt lgkmcnt(7)\n"
      "v_mfma_f32_32x32x16_bf16 v[32:47], v[170:173], v[48:51], v[32:47]\n"
      "ds_read_b128 v[154:157], v72 offset:17408\n"
      "ds_read_b64_tr_b16 v[178:179], v93 offset:20480\n"
      "ds_read_b64_tr_b16 v[180:181], v93 offset:20992\n"
      "s_waitcnt lgkmcnt(8)\n"
      "v_mfma_f32_32x32x16_bf16 v[118:133], v[48:51], v[194:197], v[118:133]\n"
      "ds_read_b64_tr_b16 v[202:203], v93 offset:21504\n"
      "ds_read_b64_tr_b16 v[204:205], v93 offset:22016\n"
      "s_waitcnt lgkmcnt(7)\n"
      "v_mfma_f32_32x32x16_bf16 v[32:47], v[174:177], v[150:153], v[32:47]\n"
      "ds_read_b128 v[48:51], v88 offset:18432\n"
      "ds_read_b64_tr_b16 v[170:171], v93 offset:24576\n"
      "ds_read_b64_tr_b16 v[172:173], v93 offset:25088\n"
      "s_waitcnt lgkmcnt(8)\n"
      "v_mfma_f32_32x32x16_bf16 v[118:133], v[150:153], v[198:201], v[118:133]\n"
      "ds_read_b64_tr_b16 v[194:195], v93 offset:25600\n"
      "ds_read_b64_tr_b16 v[196:197], v93 offset:26112\n"
      "s_waitcnt lgkmcnt(7)\n"
      "s_nop 0\n"
      ".Lpage4_%=:\n"
      "s_cbranch_execz .Lret_%=\n"
      "v_mfma_f32_32x32x16_bf16 v[32:47], v[178:181], v[154:157], v[32:47]\n"
      "ds_read_b128 v[150:153], v89 offset:19456\n"
      "ds_read_b64_tr_b16 v[174:175], v93 offset:28672\n"
      "ds_read_b64_tr_b16 v[176:177], v93 offset:29184\n"
      "s_waitcnt lgkmcnt(8)\n"
      "v_mfma_f32_32x32x16_bf16 v[118:133], v[154:157], v[202:205], v[118:133]\n"
      "ds_read_b64_tr_b16 v[198:199], v93 offset:29696\n"
      "ds_read_b64_tr_b16 v[200:201], v93 offset:30208\n"
      "s_barrier\n"
      "s_waitcnt lgkmcnt(7)\n"
      "v_mfma_f32_32x32x16_bf16 v[32:47], v[170:173], v[48:51], v[32:47]\n"
      "ds_read_b128 v[154:157], v55 offset:20480\n"
      "ds_read_b64_tr_b16 v[178:179], v93 offset:32768\n"
      "ds_read_b64_tr_b16 v[180:181], v93 offset:33280\n"
      "s_waitcnt lgkmcnt(8)\n"
      "v_mfma_f32_32x32x16_bf16 v[118:133], v[48:51], v[194:197], v[118:133]\n"
      "ds_read_b64_tr_b16 v[202:203], v93 offset:33792\n"
      "ds_read_b64_tr_b16 v[204:205], v93 offset:34304\n"
      "s_waitcnt lgkmcnt(7)\n"
      "v_mfma_f32_32x32x16_bf16 v[32:47], v[174:177], v[150:153], v[32:47]\n"
      "ds_read_b128 v[48:51], v72 offset:21504\n"
      "ds_read_b64_tr_b16 v[170:171], v93 offset:36864\n"
      "ds_read_b64_tr_b16 v[172:173], v93 offset:37376\n"
      "s_waitcnt lgkmcnt(8)\n"
      "v_mfma_f32_32x32x16_bf16 v[118:133], v[150:153], v[198:201], v[118:133]\n"
      "ds_read_b64_tr_b16 v[194:195], v93 offset:37888\n"
      "ds_read_b64_tr_b16 v[196:197], v93 offset:38400\n"
      "s_waitcnt lgkmcnt(7)\n"
      "v_mfma_f32_32x32x16_bf16 v[32:47], v[178:181], v[154:157], v[32:47]\n"
      "ds_read_b128 v[150:153], v88 offset:22528\n"
      "ds_read_b64_tr_b16 v[174:175], v93 offset:40960\n"
      "ds_read_b64_tr_b16 v[176:177], v93 offset:41472\n"
      "s_waitcnt lgkmcnt(8)\n"
      "v_mfma_f32_32x32x16_bf16 v[118:133], v[154:157], v[202:205], v[118:133]\n"
      "ds_read_b64_tr_b16 v[198:199], v93 offset:41984\n"
      "ds_read_b64_tr_b16 v[200:201], v93 offset:42496\n"
      "s_waitcnt lgkmcnt(7)\n"
      "v_mfma_f32_32x32x16_bf16 v[32:47], v[170:173], v[48:51], v[32:47]\n"
      "ds_read_b128 v[154:157], v89 offset:23552\n"
      "ds_read_b64_tr_b16 v[178:179], v93 offset:45056\n"
      "ds_read_b64_tr_b16 v[180:181], v93 offset:45568\n"
      "s_waitcnt lgkmcnt(8)\n"
      "v_mfma_f32_32x32x16_bf16 v[118:133], v[48:51], v[194:197], v[118:133]\n"
      "ds_read_b64_tr_b16 v[202:203], v93 offset:46080\n"
      "ds_read_b64_tr_b16 v[204:205], v93 offset:46592\n"
      "s_mov_b32 s60, 0x100000\n"
      "s_waitcnt vmcnt(0)\n"
      "v_readfirstlane_b32 s58, v8\n"
      ".Lann_2003_%=:\n"
      "v_readfirstlane_b32 s59, v9\n"
      "s_nop 1\n"
      "s_xor_b64 s[58:59], s[58:59], s[36:37]\n"
      "s_and_b32 s76, s58, 1\n"
      "s_lshr_b64 s[58:59], s[58:59], 1\n"
      "s_cbranch_scc0 .Lannend_2004_%=\n"
      "buffer_load_dwordx2 v[8:9], v7, s[24:27], s63 offen sc1\n"
      "s_waitcnt vmcnt(0)\n"
      "v_readfirstlane_b32 s58, v8\n"
      "s_sub_u32 s60, s60, 1\n"
      "s_cbranch_scc0 .Lann_2003_%=\n"
      ".Lannend_2004_%=:\n"
      "s_lshl_b32 s76, s76, 13\n"
      "s_lshl_b32 s59, s43, 21\n"
      "s_add_u32 s76, s76, s59\n"
      "s_add_u32 s62, s62, s76\n"
      "s_add_u32 s79, s79, s76\n"
      "s_add_u32 s72, s72, s76\n"
      "s_add_u32 s51, s51, s76\n"
      "s_add_u32 s69, s69, s76\n"
      "s_add_u32 s70, s69, 65536\n"
      "s_barrier\n"
      "s_waitcnt lgkmcnt(7)\n"
      "v_mfma_f32_32x32x16_bf16 v[32:47], v[174:177], v[150:153], v[32:47]\n"
      "ds_read_b128 v[48:51], v55 offset:24576\n"
      "ds_read_b64_tr_b16 v[170:171], v93 offset:49152\n"
      "ds_read_b64_tr_b16 v[172:173], v93 offset:49664\n"
      "s_waitcnt lgkmcnt(8)\n"
      "v_mfma_f32_32x32x16_bf16 v[118:133], v[150:153], v[198:201], v[118:133]\n"
      "ds_read_b64_tr_b16 v[194:195], v93 offset:50176\n"
      "ds_read_b64_tr_b16 v[196:197], v93 offset:50688\n"
      "s_waitcnt lgkmcnt(7)\n"
      "v_mfma_f32_32x32x16_bf16 v[32:47], v[178:181], v[154:157], v[32:47]\n"
      "ds_read_b128 v[150:153], v72 offset:25600\n"
      "ds_read_b64_tr_b16 v[174:175], v93 offset:53248\n"
      "ds_read_b64_tr_b16 v[176:177], v93 offset:53760\n"
      "s_waitcnt lgkmcnt(8)\n"
      "v_mfma_f32_32x32x16_bf16 v[118:133], v[154:157], v[202:205], v[118:133]\n"
      "ds_read_b64_tr_b16 v[198:199], v93 offset:54272\n"
      "ds_read_b64_tr_b16 v[200:201], v93 offset:54784\n"
      "s_waitcnt lgkmcnt(7)\n"
      "v_mfma_f32_32x32x16_bf16 v[32:47], v[170:173], v[48:51], v[32:47]\n"
      "ds_read_b128 v[154:157], v88 offset:26624\n"
      "ds_read_b64_tr_b16 v[178:179], v93 offset:57344\n"
      "ds_read_b64_tr_b16 v[180:181], v93 offset:57856\n"
      "s_waitcnt lgkmcnt(8)\n"
      "v_mfma_f32_32x32x16_bf16 v[118:133], v[48:51], v[194:197], v[118:133]\n"
      "ds_read_b64_tr_b16 v[202:203], v93 offset:58368\n"
      "ds_read_b64_tr_b16 v[204:205], v93 offset:58880\n"
      "s_waitcnt lgkmcnt(7)\n"
      "v_mfma_f32_32x32x16_bf16 v[32:47], v[174:177], v[150:153], v[32:47]\n"
      "ds_read_b128 v[48:51], v89 offset:27648\n"
      "ds_read_b64_tr_b16 v[170:171], v93 offset:61440\n"
      "ds_read_b64_tr_b16 v[172:173], v93 offset:61952\n"
      "s_waitcnt lgkmcnt(8)\n"
      "v_mfma_f32_32x32x16_bf16 v[118:133], v[150:153], v[198:201], v[118:133]\n"
      "ds_read_b64_tr_b16 v[194:195], v93 offset:62464\n"
      "ds_read_b64_tr_b16 v[196:197], v93 offset:62976\n"
      "s_barrier\n"
      "s_waitcnt lgkmcnt(7)\n"
      "v_mfma_f32_32x32x16_bf16 v[32:47], v[178:181], v[154:157], v[32:47]\n"
      "ds_read_b128 v[150:153], v55 offset:28672\n"
      "ds_read_b64_tr_b16 v[174:175], v94 offset:0\n"
      "ds_read_b64_tr_b16 v[176:177], v94 offset:512\n"
      "s_waitcnt lgkmcnt(8)\n"
      "v_mfma_f32_32x32x16_bf16 v[118:133], v[154:157], v[202:205], v[118:133]\n"
      "ds_read_b64_tr_b16 v[198:199], v94 offset:1024\n"
      "ds_read_b64_tr_b16 v[200:201], v94 offset:1536\n"
      "s_waitcnt lgkmcnt(7)\n"
      "v_mfma_f32_32x32x16_bf16 v[32:47], v[170:173], v[48:51], v[32:47]\n"
      "ds_read_b128 v[154:157], v72 offset:29696\n"
      "ds_read_b64_tr_b16 v[178:179], v94 offset:4096\n"
      "ds_read_b64_tr_b16 v[180:181], v94 offset:4608\n"
      "s_waitcnt lgkmcnt(8)\n"
      "v_mfma_f32_32x32x16_bf16 v[118:133], v[48:51], v[194:197], v[118:133]\n"
      "ds_read_b64_tr_b16 v[202:203], v94 offset:5120\n"
      "ds_read_b64_tr_b16 v[204:205], v94 offset:5632\n"
      "s_waitcnt lgkmcnt(7)\n"
      "v_mfma_f32_32x32x16_bf16 v[32:47], v[174:177], v[150:153], v[32:47]\n"
      "ds_read_b128 v[48:51], v88 offset:30720\n"
      "ds_read_b64_tr_b16 v[170:171], v94 offset:8192\n"
      "ds_read_b64_tr_b16 v[172:173], v94 offset:8704\n"
      "s_waitcnt lgkmcnt(8)\n"
      "v_mfma_f32_32x32x16_bf16 v[118:133], v[150:153], v[198:201], v[118:133]\n"
      "ds_read_b64_tr_b16 v[194:195], v94 offset:9216\n"
      "ds_read_b64_tr_b16 v[196:197], v94 offset:9728\n"
      "s_waitcnt lgkmcnt(7)\n"
      "v_mfma_f32_32x32x16_bf16 v[32:47], v[178:181], v[154:157], v[32:47]\n"
      "ds_read_b128 v[150:153], v89 offset:31744\n"
      "ds_read_b64_tr_b16 v[174:175], v94 offset:12288\n"
      "ds_read_b64_tr_b16 v[176:177], v94 offset:12800\n"
      "s_waitcnt lgkmcnt(8)\n"
      "v_mfma_f32_32x32x16_bf16 v[118:133], v[154:157], v[202:205], v[118:133]\n"
      "ds_read_b64_tr_b16 v[198:199], v94 offset:13312\n"
      "ds_read_b64_tr_b16 v[200:201], v94 offset:13824\n"
      "s_waitcnt lgkmcnt(7)\n"
      "v_mfma_f32_32x32x16_bf16 v[32:47], v[170:173], v[48:51], v[32:47]\n"
      "s_waitcnt lgkmcnt(5)\n"
      "v_mfma_f32_32x32x16_bf16 v[118:133], v[48:51], v[194:197], v[118:133]\n"
      "s_waitcnt lgkmcnt(2)\n"
      "v_mfma_f32_32x32x16_bf16 v[32:47], v[174:177], v[150:153], v[32:47]\n"
      "s_waitcnt lgkmcnt(0)\n"
      "v_mfma_f32_32x32x16_bf16 v[118:133], v[150:153], v[198:201], v[118:133]\n"
      "s_nop 9\n"
      "v_cvt_pk_bf16_f32 v8, v32, v33\n"
      "v_cvt_pk_bf16_f32 v9, v34, v35\n"
      "v_cvt_pk_bf16_f32 v10, v36, v37\n"
      "v_cvt_pk_bf16_f32 v11, v38, v39\n"
      "buffer_store_dwordx4 v[8:11], v2, s[24:27], s62 offen sc1\n"
      "v_cvt_pk_bf16_f32 v114, v40, v41\n"
      "v_cvt_pk_bf16_f32 v115, v42, v43\n"
      "v_cvt_pk_bf16_f32 v116, v44, v45\n"
      "v_cvt_pk_bf16_f32 v117, v46, v47\n"
      "buffer_store_dwordx4 v[114:117], v2, s[24:27], s62 offen offset:1024 sc1\n"
      "v_cvt_pk_bf16_f32 v206, v118, v119\n"
      "v_cvt_pk_bf16_f32 v207, v120, v121\n"
      "v_cvt_pk_bf16_f32 v208, v122, v123\n"
      "v_cvt_pk_bf16_f32 v209, v124, v125\n"
      "buffer_store_dwordx4 v[206:209], v2, s[24:27], s62 offen offset:2048 sc1\n"
      "v_cvt_pk_bf16_f32 v210, v126, v127\n"
      "v_cvt_pk_bf16_f32 v211, v128, v129\n"
      "v_cvt_pk_bf16_f32 v212, v130, v131\n"
      "v_cvt_pk_bf16_f32 v213, v132, v133\n"
      "buffer_store_dwordx4 v[210:213], v2, s[24:27], s62 offen offset:3072 sc1\n"
      "s_barrier\n"
      "ds_read_b128 v[48:51], v55 offset:0\n"
      "ds_read_b64_tr_b16 v[170:171], v93 offset:0\n"
      "ds_read_b64_tr_b16 v[172:173], v93 offset:512\n"
      "ds_read_b128 v[150:153], v72 offset:1024\n"
      "ds_read_b64_tr_b16 v[174:175], v93 offset:4096\n"
      "ds_read_b64_tr_b16 v[176:177], v93 offset:4608\n"
      "ds_read_b128 v[154:157], v88 offset:2048\n"
      "ds_read_b64_tr_b16 v[178:179], v93 offset:8192\n"
      "ds_read_b64_tr_b16 v[180:181], v93 offset:8704\n"
      "ds_read_b128 v[158:161], v89 offset:3072\n"
      "ds_read_b64_tr_b16 v[182:183], v93 offset:12288\n"
      "ds_read_b64_tr_b16 v[184:185], v93 offset:12800\n"
      "s_waitcnt lgkmcnt(9)\n"
      "v_mfma_f32_32x32x16_bf16 v[134:149], v[170:173], v[48:51], 0\n"
      "ds_read_b128 v[162:165], v55 offset:4096\n"
      "ds_read_b64_tr_b16 v[186:187], v93 offset:1024\n"
      "ds_read_b64_tr_b16 v[188:189], v93 offset:1536\n"
      "s_waitcnt lgkmcnt(9)\n"
      "v_mfma_f32_32x32x16_bf16 v[134:149], v[174:177], v[150:153], v[134:149]\n"
      "ds_read_b128 v[166:169], v72 offset:5120\n"
      "ds_read_b64_tr_b16 v[190:191], v93 offset:5120\n"
      "ds_read_b64_tr_b16 v[192:193], v93 offset:5632\n"
      "s_waitcnt lgkmcnt(9)\n"
      "v_mfma_f32_32x32x16_bf16 v[134:149], v[178:181], v[154:157], v[134:149]\n"
      "ds_read_b128 v[48:51], v88 offset:6144\n"
      "ds_read_b64_tr_b16 v[170:171], v93 offset:9216\n"
      "ds_read_b64_tr_b16 v[172:173], v93 offset:9728\n"
      "s_waitcnt lgkmcnt(9)\n"
      "v_mfma_f32_32x32x16_bf16 v[134:149], v[182:185], v[158:161], v[134:149]\n"
      "ds_read_b128 v[150:153], v89 offset:7168\n"
      "ds_read_b64_tr_b16 v[174:175], v93 offset:13312\n"
      "ds_read_b64_tr_b16 v[176:177], v93 offset:13824\n"
      "ds_write_b128 v103, v[8:11] offset:0\n"
      "ds_write_b128 v103, v[114:117] offset:1024\n"
      "ds_write_b128 v103, v[206:209] offset:2048\n"
      "ds_write_b128 v103, v[210:213] offset:3072\n"
      "s_barrier\n"
      "s_waitcnt lgkmcnt(13)\n"
      "v_mfma_f32_32x32x16_bf16 v[134:149], v[186:189], v[162:165], v[134:149]\n"
      "ds_read_b128 v[154:157], v55 offset:8192\n"
      "ds_read_b64_tr_b16 v[178:179], v93 offset:16384\n"
      "ds_read_b64_tr_b16 v[180:181], v93 offset:16896\n"
      "s_waitcnt lgkmcnt(13)\n"
      "v_mfma_f32_32x32x16_bf16 v[134:149], v[190:193], v[166:169], v[134:149]\n"
      "ds_read_b128 v[158:161], v72 offset:9216\n"
      "ds_read_b64_tr_b16 v[182:183], v93 offset:20480\n"
      "ds_read_b64_tr_b16 v[184:185], v93 offset:20992\n"
      "s_waitcnt lgkmcnt(13)\n"
      "v_mfma_f32_32x32x16_bf16 v[134:149], v[170:173], v[48:51], v[134:149]\n"
      "ds_read_b128 v[162:165], v88 offset:10240\n"
      "ds_read_b64_tr_b16 v[186:187], v93 offset:24576\n"
      "ds_read_b64_tr_b16 v[188:189], v93 offset:25088\n"
      "s_waitcnt lgkmcnt(13)\n"
      "v_mfma_f32_32x32x16_bf16 v[134:149], v[174:177], v[150:153], v[134:149]\n"
      "ds_read_b128 v[166:169], v89 offset:11264\n"
      "ds_read_b64_tr_b16 v[190:191], v93 offset:28672\n"
      "ds_read_b64_tr_b16 v[192:193], v93 offset:29184\n"
      "s_waitcnt lgkmcnt(9)\n"
      "v_mfma_f32_32x32x16_bf16 v[134:149], v[178:181], v[154:157], v[134:149]\n"
      "ds_read_b128 v[48:51], v55 offset:12288\n"
      "ds_read_b64_tr_b16 v[170:171], v93 offset:17408\n"
      "ds_read_b64_tr_b16 v[172:173], v93 offset:17920\n"
      "s_waitcnt lgkmcnt(9)\n"
      "v_mfma_f32_32x32x16_bf16 v[134:149], v[182:185], v[158:161], v[134:149]\n"
      "ds_read_b128 v[150:153], v72 offset:13312\n"
      "ds_read_b64_tr_b16 v[174:175], v93 offset:21504\n"
      "ds_read_b64_tr_b16 v[176:177], v93 offset:22016\n"
      "s_waitcnt lgkmcnt(9)\n"
      "v_mfma_f32_32x32x16_bf16 v[134:149], v[186:189], v[162:165], v[134:149]\n"
      "ds_read_b128 v[154:157], v88 offset:14336\n"
      "ds_read_b64_tr_b16 v[178:179], v93 offset:25600\n"
      "ds_read_b64_tr_b16 v[180:181], v93 offset:26112\n"
      "s_waitcnt lgkmcnt(9)\n"
      "v_mfma_f32_32x32x16_bf16 v[134:149], v[190:193], v[166:169], v[134:149]\n"
      "ds_read_b128 v[158:161], v89 offset:15360\n"
      "ds_read_b64_tr_b16 v[182:183], v93 offset:29696\n"
      "ds_read_b64_tr_b16 v[184:185], v93 offset:30208\n"
      "s_waitcnt vmcnt(0)\n"
      "s_barrier\n"
      "buffer_store_dwordx4 v[96:99], v95, s[24:27], s79 offen sc1\n"
      "s_waitcnt lgkmcnt(9)\n"
      "v_mfma_f32_32x32x16_bf16 v[134:149], v[170:173], v[48:51], v[134:149]\n"
      "ds_read_b128 v[162:165], v55 offset:16384\n"
      "ds_read_b64_tr_b16 v[186:187], v93 offset:32768\n"
      "ds_read_b64_tr_b16 v[188:189], v93 offset:33280\n"
      "s_waitcnt lgkmcnt(9)\n"
      "v_mfma_f32_32x32x16_bf16 v[134:149], v[174:177], v[150:153], v[134:149]\n"
      "ds_read_b128 v[166:169], v72 offset:17408\n"
      "ds_read_b64_tr_b16 v[190:191], v93 offset:36864\n"
      "ds_read_b64_tr_b16 v[192:193], v93 offset:37376\n"
      "s_waitcnt lgkmcnt(9)\n"
      "v_mfma_f32_32x32x16_bf16 v[134:149], v[178:181], v[154:157], v[134:149]\n"
      "ds_read_b128 v[48:51], v88 offset:18432\n"
      "ds_read_b64_tr_b16 v[170:171], v93 offset:40960\n"
      "ds_read_b64_tr_b16 v[172:173], v93 offset:41472\n"
      "s_waitcnt lgkmcnt(9)\n"
      "v_mfma_f32_32x32x16_bf16 v[134:149], v[182:185], v[158:161], v[134:149]\n"
      "ds_read_b128 v[150:153], v89 offset:19456\n"
      "ds_read_b64_tr_b16 v[174:175], v93 offset:45056\n"
      "ds_read_b64_tr_b16 v[176:177], v93 offset:45568\n"
      "s_waitcnt lgkmcnt(9)\n"
      "v_mfma_f32_32x32x16_bf16 v[134:149], v[186:189], v[162:165], v[134:149]\n"
      "ds_read_b128 v[154:157], v55 offset:20480\n"
      "ds_read_b64_tr_b16 v[178:179], v93 offset:33792\n"
      "ds_read_b64_tr_b16 v[180:181], v93 offset:34304\n"
      "s_waitcnt lgkmcnt(9)\n"
      "v_mfma_f32_32x32x16_bf16 v[134:149], v[190:193], v[166:169], v[134:149]\n"
      "ds_read_b128 v[158:161], v72 offset:21504\n"
      "ds_read_b64_tr_b16 v[182:183], v93 offset:37888\n"
      "ds_read_b64_tr_b16 v[184:185], v93 offset:38400\n"
      "s_waitcnt lgkmcnt(9)\n"
      "v_mfma_f32_32x32x16_bf16 v[134:149], v[170:173], v[48:51], v[134:149]\n"
      "ds_read_b128 v[162:165], v88 offset:22528\n"
      "ds_read_b64_tr_b16 v[186:187], v93 offset:41984\n"
      "ds_read_b64_tr_b16 v[188:189], v93 offset:42496\n"
      "s_waitcnt lgkmcnt(9)\n"
      "v_mfma_f32_32x32x16_bf16 v[134:149], v[174:177], v[150:153], v[134:149]\n"
      "ds_read_b128 v[166:169], v89 offset:23552\n"
      "ds_read_b64_tr_b16 v[190:191], v93 offset:46080\n"
      "ds_read_b64_tr_b16 v[192:193], v93 offset:46592\n"
      "s_barrier\n"
      "buffer_store_dwordx4 v[28:31], v101, s[24:27], s72 offen sc1\n"
      "buffer_store_dwordx4 v[28:31], v102, s[24:27], s51 offen sc1\n"
      "s_add_u32 s51, s51, 65536\n"
      "buffer_store_dwordx4 v[28:31], v102, s[24:27], s51 offen sc1\n"
      "s_add_u32 s51, s51, -65536\n"
      "s_waitcnt lgkmcnt(9)\n"
      "v_mfma_f32_32x32x16_bf16 v[134:149], v[178:181], v[154:157], v[134:149]\n"
      "ds_read_b128 v[48:51], v55 offset:24576\n"
      "ds_read_b64_tr_b16 v[170:171], v93 offset:49152\n"
      "ds_read_b64_tr_b16 v[172:173], v93 offset:49664\n"
      "s_waitcnt lgkmcnt(9)\n"
      "v_mfma_f32_32x32x16_bf16 v[134:149], v[182:185], v[158:161], v[134:149]\n"
      "ds_read_b128 v[150:153], v72 offset:25600\n"
      "ds_read_b64_tr_b16 v[174:175], v93 offset:53248\n"
      "ds_read_b64_tr_b16 v[176:177], v93 offset:53760\n"
      "s_waitcnt lgkmcnt(9)\n"
      "v_mfma_f32_32x32x16_bf16 v[134:149], v[186:189], v[162:165], v[134:149]\n"
      "ds_read_b128 v[154:157], v88 offset:26624\n"
      "ds_read_b64_tr_b16 v[178:179], v93 offset:57344\n"
      "ds_read_b64_tr_b16 v[180:181], v93 offset:57856\n"
      "s_waitcnt lgkmcnt(9)\n"
      "v_mfma_f32_32x32x16_bf16 v[134:149], v[190:193], v[166:169], v[134:149]\n"
      "ds_read_b128 v[158:161], v89 offset:27648\n"
      "ds_read_b64_tr_b16 v[182:183], v93 offset:61440\n"
      "ds_read_b64_tr_b16 v[184:185], v93 offset:61952\n"
      "s_waitcnt lgkmcnt(9)\n"
      "v_mfma_f32_32x32x16_bf16 v[134:149], v[170:173], v[48:51], v[134:149]\n"
      "ds_read_b128 v[162:165], v55 offset:28672\n"
      "ds_read_b64_tr_b16 v[186:187], v93 offset:50176\n"
      "ds_read_b64_tr_b16 v[188:189], v93 offset:50688\n"
      "s_waitcnt lgkmcnt(9)\n"
      "v_mfma_f32_32x32x16_bf16 v[134:149], v[174:177], v[150:153], v[134:149]\n"
      "ds_read_b128 v[166:169], v72 offset:29696\n"
      "ds_read_b64_tr_b16 v[190:191], v93 offset:54272\n"
      "ds_read_b64_tr_b16 v[192:193], v93 offset:54784\n"
      "s_waitcnt lgkmcnt(9)\n"
      "v_mfma_f32_32x32x16_bf16 v[134:149], v[178:181], v[154:157], v[134:149]\n"
      "ds_read_b128 v[48:51], v88 offset:30720\n"
      "ds_read_b64_tr_b16 v[170:171], v93 offset:58368\n"
      "ds_read_b64_tr_b16 v[172:173], v93 offset:58880\n"
      "s_waitcnt lgkmcnt(9)\n"
      "v_mfma_f32_32x32x16_bf16 v[134:149], v[182:185], v[158:161], v[134:149]\n"
      "ds_read_b128 v[150:153], v89 offset:31744\n"
      "ds_read_b64_tr_b16 v[174:175], v93 offset:62464\n"
      "ds_read_b64_tr_b16 v[176:177], v93 offset:62976\n"
      "s_waitcnt lgkmcnt(9)\n"
      "v_mfma_f32_32x32x16_bf16 v[134:149], v[186:189], v[162:165], v[134:149]\n"
      "s_waitcnt lgkmcnt(6)\n"
      "v_mfma_f32_32x32x16_bf16 v[134:149], v[190:193], v[166:169], v[134:149]\n"
      "s_waitcnt lgkmcnt(3)\n"
      "v_mfma_f32_32x32x16_bf16 v[134:149], v[170:173], v[48:51], v[134:149]\n"
      "s_waitcnt lgkmcnt(0)\n"
      "v_mfma_f32_32x32x16_bf16 v[134:149], v[174:177], v[150:153], v[134:149]\n"
      "v_mov_b32 v52, 0x3eb8aa3b\n"
      "s_nop 10\n"
      "v_pk_mul_f32 v[134:135], v[134:135], v[52:53] op_sel_hi:[1,0]\n"
      "v_pk_mul_f32 v[136:137], v[136:137], v[52:53] op_sel_hi:[1,0]\n"
      "v_pk_mul_f32 v[138:139], v[138:139], v[52:53] op_sel_hi:[1,0]\n"
      "v_pk_mul_f32 v[140:141], v[140:141], v[52:53] op_sel_hi:[1,0]\n"
      "v_cvt_pk_bf16_f32 v8, v134, v135\n"
      "v_cvt_pk_bf16_f32 v9, v136, v137\n"
      "v_cvt_pk_bf16_f32 v10, v138, v139\n"
      "v_cvt_pk_bf16_f32 v11, v140, v141\n"
      "ds_write_b128 v104, v[8:11] offset:0\n"
      "v_pk_mul_f32 v[142:143], v[142:143], v[52:53] op_sel_hi:[1,0]\n"
      "v_pk_mul_f32 v[144:145], v[144:145], v[52:53] op_sel_hi:[1,0]\n"
      "v_pk_mul_f32 v[146:147], v[146:147], v[52:53] op_sel_hi:[1,0]\n"
      "v_pk_mul_f32 v[148:149], v[148:149], v[52:53] op_sel_hi:[1,0]\n"
      "v_cvt_pk_bf16_f32 v114, v142, v143\n"
      "v_cvt_pk_bf16_f32 v115, v144, v145\n"
      "v_cvt_pk_bf16_f32 v116, v146, v147\n"
      "v_cvt_pk_bf16_f32 v117, v148, v149\n"
      "ds_write_b128 v104, v[114:117] offset:1024\n"
      "s_waitcnt lgkmcnt(0)\n"
      "s_barrier\n"
      "ds_read_b128 v[44:47], v76 offset:4096\n"
      "ds_read_b128 v[8:11], v76 offset:0\n"
      "ds_read_b128 v[48:51], v74 offset:8192\n"
      "ds_read_b128 v[52:55], v74 offset:0\n"
      "ds_read_b128 v[162:165], v87 offset:0\n"
      "ds_read_b128 v[166:169], v87 offset:1024\n"
      "ds_read_b128 v[170:173], v86 offset:0\n"
      "ds_read_b128 v[174:177], v86 offset:1024\n"
      "s_waitcnt lgkmcnt(5)\n"
      "v_mfma_f32_32x32x16_bf16 v[28:43], v[48:51], v[44:47], v[56:71]\n"
      "s_nop 11\n"
      "v_exp_f32 v28, v28\n"
      "v_exp_f32 v29, v29\n"
      "v_exp_f32 v30, v30\n"
      "v_exp_f32 v31, v31\n"
      "v_exp_f32 v32, v32\n"
      "v_cvt_pk_bf16_f32 v178, v28, v29\n"
      "v_exp_f32 v33, v33\n"
      "s_waitcnt lgkmcnt(4)\n"
      "v_mfma_f32_32x32x16_bf16 v[114:129], v[52:55], v[44:47], 0\n"
      "v_cvt_pk_bf16_f32 v179, v30, v31\n"
      "v_exp_f32 v34, v34\n"
      "v_exp_f32 v35, v35\n"
      "v_exp_f32 v36, v36\n"
      "v_cvt_pk_bf16_f32 v180, v32, v33\n"
      "v_exp_f32 v37, v37\n"
      "v_cvt_pk_bf16_f32 v181, v34, v35\n"
      "v_exp_f32 v38, v38\n"
      "v_exp_f32 v39, v39\n"
      "s_waitcnt lgkmcnt(3)\n"
      "v_mfma_f32_32x32x16_bf16 v[146:161], v[162:165], v[178:181], 0\n"
      "v_cvt_pk_bf16_f32 v182, v36, v37\n"
      "v_exp_f32 v40, v40\n"
      "v_exp_f32 v41, v41\n"
      "v_cvt_pk_bf16_f32 v183, v38, v39\n"
      "v_exp_f32 v42, v42\n"
      "v_exp_f32 v43, v43\n"
      "v_cvt_pk_bf16_f32 v184, v40, v41\n"
      "v_cvt_pk_bf16_f32 v185, v42, v43\n"
      "v_exp_f32 v114, v114\n"
      "v_exp_f32 v115, v115\n"
      "s_waitcnt lgkmcnt(2)\n"
      "v_mfma_f32_32x32x16_bf16 v[146:161], v[166:169], v[182:185], v[146:161]\n"
      "v_exp_f32 v116, v116\n"
      "v_exp_f32 v117, v117\n"
      "v_exp_f32 v118, v118\n"
      "v_cvt_pk_bf16_f32 v178, v114, v115\n"
      "v_exp_f32 v119, v119\n"
      "v_mfma_f32_32x32x16_bf16 v[28:43], v[52:55], v[8:11], v[56:71]\n"
      "v_cvt_pk_bf16_f32 v179, v116, v117\n"
      "v_exp_f32 v120, v120\n"
      "v_exp_f32 v121, v121\n"
      "v_exp_f32 v122, v122\n"
      "v_cvt_pk_bf16_f32 v180, v118, v119\n"
      "v_exp_f32 v123, v123\n"
      "v_cvt_pk_bf16_f32 v181, v120, v121\n"
      "v_exp_f32 v124, v124\n"
      "v_exp_f32 v125, v125\n"
      "s_waitcnt lgkmcnt(1)\n"
      "v_mfma_f32_32x32x16_bf16 v[146:161], v[170:173], v[178:181], v[146:161]\n"
      "v_cvt_pk_bf16_f32 v182, v122, v123\n"
      "v_exp_f32 v126, v126\n"
      "v_exp_f32 v127, v127\n"
      "v_cvt_pk_bf16_f32 v183, v124, v125\n"
      "v_exp_f32 v128, v128\n"
      "v_exp_f32 v129, v129\n"
      "v_cvt_pk_bf16_f32 v184, v126, v127\n"
      "v_cvt_pk_bf16_f32 v185, v128, v129\n"
      "v_exp_f32 v28, v28\n"
      "v_exp_f32 v29, v29\n"
      "s_waitcnt lgkmcnt(0)\n"
      "v_mfma_f32_32x32x16_bf16 v[146:161], v[174:177], v[182:185], v[146:161]\n"
      "v_exp_f32 v30, v30\n"
      "v_exp_f32 v31, v31\n"
      "v_exp_f32 v32, v32\n"
      "v_cvt_pk_bf16_f32 v178, v28, v29\n"
      "v_exp_f32 v33, v33\n"
      "v_cvt_pk_bf16_f32 v179, v30, v31\n"
      "v_exp_f32 v34, v34\n"
      "v_exp_f32 v35, v35\n"
      "v_exp_f32 v36, v36\n"
      "v_cvt_pk_bf16_f32 v180, v32, v33\n"
      "v_exp_f32 v37, v37\n"
      "v_cvt_pk_bf16_f32 v181, v34, v35\n"
      "v_exp_f32 v38, v38\n"
      "v_exp_f32 v39, v39\n"
      "v_mfma_f32_32x32x16_bf16 v[130:145], v[170:173], v[178:181], 0\n"
      "v_cvt_pk_bf16_f32 v182, v36, v37\n"
      "v_exp_f32 v40, v40\n"
      "v_exp_f32 v41, v41\n"
      "v_cvt_pk_bf16_f32 v183, v38, v39\n"
      "v_exp_f32 v42, v42\n"
      "v_exp_f32 v43, v43\n"
      "v_cvt_pk_bf16_f32 v184, v40, v41\n"
      "v_cvt_pk_bf16_f32 v185, v42, v43\n"
      "s_nop 1\n"
      "v_mfma_f32_32x32x16_bf16 v[130:145], v[174:177], v[182:185], v[130:145]\n"
      "s_barrier\n"
      "ds_read_b128 v[48:51], v73 offset:8192\n"
      "ds_read_b128 v[52:55], v73 offset:24576\n"
      "ds_read_b128 v[162:165], v79 offset:0\n"
      "ds_read_b128 v[166:169], v79 offset:1024\n"
      "ds_read_b128 v[170:173], v81 offset:0\n"
      "ds_read_b128 v[174:177], v81 offset:1024\n"
      "s_waitcnt lgkmcnt(5)\n"
      "v_mfma_f32_32x32x16_bf16 v[114:129], v[48:51], v[44:47], v[12:27]\n"
      "s_nop 11\n"
      "v_exp_f32 v114, v114\n"
      "v_exp_f32 v115, v115\n"
      "v_exp_f32 v116, v116\n"
      "v_exp_f32 v117, v117\n"
      "v_exp_f32 v118, v118\n"
      "v_cvt_pk_bf16_f32 v178, v114, v115\n"
      "v_exp_f32 v119, v119\n"
      "v_mfma_f32_32x32x16_bf16 v[28:43], v[48:51], v[8:11], 0\n"
      "v_cvt_pk_bf16_f32 v179, v116, v117\n"
      "v_exp_f32 v120, v120\n"
      "v_exp_f32 v121, v121\n"
      "v_exp_f32 v122, v122\n"
      "v_cvt_pk_bf16_f32 v180, v118, v119\n"
      "v_exp_f32 v123, v123\n"
      "v_cvt_pk_bf16_f32 v181, v120, v121\n"
      "v_exp_f32 v124, v124\n"
      "v_exp_f32 v125, v125\n"
      "s_waitcnt lgkmcnt(3)\n"
      "v_mfma_f32_32x32x16_bf16 v[146:161], v[162:165], v[178:181], v[146:161]\n"
      "v_cvt_pk_bf16_f32 v182, v122, v123\n"
      "v_exp_f32 v126, v126\n"
      "v_exp_f32 v127, v127\n"
      "v_cvt_pk_bf16_f32 v183, v124, v125\n"
      "v_exp_f32 v128, v128\n"
      "v_exp_f32 v129, v129\n"
      "v_cvt_pk_bf16_f32 v184, v126, v127\n"
      "v_cvt_pk_bf16_f32 v185, v128, v129\n"
      "v_exp_f32 v28, v28\n"
      "v_exp_f32 v29, v29\n"
      "s_waitcnt lgkmcnt(2)\n"
      "v_mfma_f32_32x32x16_bf16 v[146:161], v[166:169], v[182:185], v[146:161]\n"
      "v_exp_f32 v30, v30\n"
      "v_exp_f32 v31, v31\n"
      "v_exp_f32 v32, v32\n"
      "v_cvt_pk_bf16_f32 v178, v28, v29\n"
      "v_exp_f32 v33, v33\n"
      "v_mfma_f32_32x32x16_bf16 v[114:129], v[52:55], v[44:47], 0\n"
      "v_cvt_pk_bf16_f32 v179, v30, v31\n"
      "v_exp_f32 v34, v34\n"
      "v_exp_f32 v35, v35\n"
      "v_exp_f32 v36, v36\n"
      "v_cvt_pk_bf16_f32 v180, v32, v33\n"
      "v_exp_f32 v37, v37\n"
      "v_cvt_pk_bf16_f32 v181, v34, v35\n"
      "v_exp_f32 v38, v38\n"
      "v_exp_f32 v39, v39\n"
      "v_mfma_f32_32x32x16_bf16 v[130:145], v[162:165], v[178:181], v[130:145]\n"
      "v_cvt_pk_bf16_f32 v182, v36, v37\n"
      "v_exp_f32 v40, v40\n"
      "v_exp_f32 v41, v41\n"
      "v_cvt_pk_bf16_f32 v183, v38, v39\n"
      "v_exp_f32 v42, v42\n"
      "v_exp_f32 v43, v43\n"
      "v_cvt_pk_bf16_f32 v184, v40, v41\n"
      "v_cvt_pk_bf16_f32 v185, v42, v43\n"
      "v_exp_f32 v114, v114\n"
      "v_exp_f32 v115, v115\n"
      "v_mfma_f32_32x32x16_bf16 v[130:145], v[166:169], v[182:185], v[130:145]\n"
      "s_waitcnt lgkmcnt(0)\n"
      "ds_read_b128 v[48:51], v73 offset:40960\n"
      "ds_read_b128 v[162:165], v83 offset:0\n"
      "ds_read_b128 v[166:169], v83 offset:1024\n"
      "v_exp_f32 v116, v116\n"
      "v_exp_f32 v117, v117\n"
      "v_exp_f32 v118, v118\n"
      "v_cvt_pk_bf16_f32 v178, v114, v115\n"
      "v_exp_f32 v119, v119\n"
      "v_mfma_f32_32x32x16_bf16 v[28:43], v[52:55], v[8:11], 0\n"
      "v_cvt_pk_bf16_f32 v179, v116, v117\n"
      "v_exp_f32 v120, v120\n"
      "v_exp_f32 v121, v121\n"
      "v_exp_f32 v122, v122\n"
      "v_cvt_pk_bf16_f32 v180, v118, v119\n"
      "v_exp_f32 v123, v123\n"
      "v_cvt_pk_bf16_f32 v181, v120, v121\n"
      "v_exp_f32 v124, v124\n"
      "v_exp_f32 v125, v125\n"
      "v_mfma_f32_32x32x16_bf16 v[146:161], v[170:173], v[178:181], v[146:161]\n"
      "v_cvt_pk_bf16_f32 v182, v122, v123\n"
      "v_exp_f32 v126, v126\n"
      "v_exp_f32 v127, v127\n"
      "v_cvt_pk_bf16_f32 v183, v124, v125\n"
      "v_exp_f32 v128, v128\n"
      "v_exp_f32 v129, v129\n"
      "v_cvt_pk_bf16_f32 v184, v126, v127\n"
      "v_cvt_pk_bf16_f32 v185, v128, v129\n"
      "v_exp_f32 v28, v28\n"
      "v_exp_f32 v29, v29\n"
      "v_mfma_f32_32x32x16_bf16 v[146:161], v[174:177], v[182:185], v[146:161]\n"
      "v_exp_f32 v30, v30\n"
      "v_exp_f32 v31, v31\n"
      "v_exp_f32 v32, v32\n"
      "v_cvt_pk_bf16_f32 v178, v28, v29\n"
      "v_exp_f32 v33, v33\n"
      "s_waitcnt lgkmcnt(2)\n"
      "v_mfma_f32_32x32x16_bf16 v[114:129], v[48:51], v[44:47], 0\n"
      "v_cvt_pk_bf16_f32 v179, v30, v31\n"
      "v_exp_f32 v34, v34\n"
      "v_exp_f32 v35, v35\n"
      "v_exp_f32 v36, v36\n"
      "v_cvt_pk_bf16_f32 v180, v32, v33\n"
      "v_exp_f32 v37, v37\n"
      "v_cvt_pk_bf16_f32 v181, v34, v35\n"
      "v_exp_f32 v38, v38\n"
      "v_exp_f32 v39, v39\n"
      "v_mfma_f32_32x32x16_bf16 v[130:145], v[170:173], v[178:181], v[130:145]\n"
      "v_cvt_pk_bf16_f32 v182, v36, v37\n"
      "v_exp_f32 v40, v40\n"
      "v_exp_f32 v41, v41\n"
      "v_cvt_pk_bf16_f32 v183, v38, v39\n"
      "v_exp_f32 v42, v42\n"
      "v_exp_f32 v43, v43\n"
      "v_cvt_pk_bf16_f32 v184, v40, v41\n"
      "v_cvt_pk_bf16_f32 v185, v42, v43\n"
      "v_exp_f32 v114, v114\n"
      "v_exp_f32 v115, v115\n"
      "v_mfma_f32_32x32x16_bf16 v[130:145], v[174:177], v[182:185], v[130:145]\n"
      "s_waitcnt lgkmcnt(0)\n"
      "ds_read_b128 v[52:55], v73 offset:57344\n"
      "ds_read_b128 v[170:173], v85 offset:0\n"
      "ds_read_b128 v[174:177], v85 offset:1024\n"
      "v_exp_f32 v116, v116\n"
      "v_exp_f32 v117, v117\n"
      "v_exp_f32 v118, v118\n"
      "v_cvt_pk_bf16_f32 v178, v114, v115\n"
      "v_exp_f32 v119, v119\n"
      "v_mfma_f32_32x32x16_bf16 v[28:43], v[48:51], v[8:11], 0\n"
      "v_cvt_pk_bf16_f32 v179, v116, v117\n"
      "v_exp_f32 v120, v120\n"
      "v_exp_f32 v121, v121\n"
      "v_exp_f32 v122, v122\n"
      "v_cvt_pk_bf16_f32 v180, v118, v119\n"
      "v_exp_f32 v123, v123\n"
      "v_cvt_pk_bf16_f32 v181, v120, v121\n"
      "v_exp_f32 v124, v124\n"
      "v_exp_f32 v125, v125\n"
      "v_mfma_f32_32x32x16_bf16 v[146:161], v[162:165], v[178:181], v[146:161]\n"
      "v_cvt_pk_bf16_f32 v182, v122, v123\n"
      "v_exp_f32 v126, v126\n"
      "v_exp_f32 v127, v127\n"
      "v_cvt_pk_bf16_f32 v183, v124, v125\n"
      "v_exp_f32 v128, v128\n"
      "v_exp_f32 v129, v129\n"
      "v_cvt_pk_bf16_f32 v184, v126, v127\n"
      "v_cvt_pk_bf16_f32 v185, v128, v129\n"
      "v_exp_f32 v28, v28\n"
      "v_exp_f32 v29, v29\n"
      "v_mfma_f32_32x32x16_bf16 v[146:161], v[166:169], v[182:185], v[146:161]\n"
      "v_exp_f32 v30, v30\n"
      "v_exp_f32 v31, v31\n"
      "v_exp_f32 v32, v32\n"
      "v_cvt_pk_bf16_f32 v178, v28, v29\n"
      "v_exp_f32 v33, v33\n"
      "s_waitcnt lgkmcnt(2)\n"
      "v_mfma_f32_32x32x16_bf16 v[114:129], v[52:55], v[44:47], 0\n"
      "v_cvt_pk_bf16_f32 v179, v30, v31\n"
      "v_exp_f32 v34, v34\n"
      "v_exp_f32 v35, v35\n"
      "v_exp_f32 v36, v36\n"
      "v_cvt_pk_bf16_f32 v180, v32, v33\n"
      ".Lpage5_%=:\n"
      "s_cbranch_execz .Lret_%=\n"
      "v_exp_f32 v37, v37\n"
      "v_cvt_pk_bf16_f32 v181, v34, v35\n"
      "v_exp_f32 v38, v38\n"
      "v_exp_f32 v39, v39\n"
      "v_mfma_f32_32x32x16_bf16 v[130:145], v[162:165], v[178:181], v[130:145]\n"
      "v_cvt_pk_bf16_f32 v182, v36, v37\n"
      "v_exp_f32 v40, v40\n"
      "v_exp_f32 v41, v41\n"
      "v_cvt_pk_bf16_f32 v183, v38, v39\n"
      "v_exp_f32 v42, v42\n"
      "v_exp_f32 v43, v43\n"
      "v_cvt_pk_bf16_f32 v184, v40, v41\n"
      "v_cvt_pk_bf16_f32 v185, v42, v43\n"
      "v_exp_f32 v114, v114\n"
      "v_exp_f32 v115, v115\n"
      "v_mfma_f32_32x32x16_bf16 v[130:145], v[166:169], v[182:185], v[130:145]\n"
      "s_waitcnt lgkmcnt(0)\n"
      "s_barrier\n"
      "ds_read_b128 v[48:51], v73 offset:16384\n"
      "ds_read_b128 v[162:165], v80 offset:0\n"
      "ds_read_b128 v[166:169], v80 offset:1024\n"
      "v_exp_f32 v116, v116\n"
      "v_exp_f32 v117, v117\n"
      "v_exp_f32 v118, v118\n"
      "v_cvt_pk_bf16_f32 v178, v114, v115\n"
      "v_exp_f32 v119, v119\n"
      "v_mfma_f32_32x32x16_bf16 v[28:43], v[52:55], v[8:11], 0\n"
      "v_cvt_pk_bf16_f32 v179, v116, v117\n"
      "v_exp_f32 v120, v120\n"
      "v_exp_f32 v121, v121\n"
      "v_exp_f32 v122, v122\n"
      "v_cvt_pk_bf16_f32 v180, v118, v119\n"
      "v_exp_f32 v123, v123\n"
      "v_cvt_pk_bf16_f32 v181, v120, v121\n"
      "v_exp_f32 v124, v124\n"
      "v_exp_f32 v125, v125\n"
      "v_mfma_f32_32x32x16_bf16 v[146:161], v[170:173], v[178:181], v[146:161]\n"
      "v_cvt_pk_bf16_f32 v182, v122, v123\n"
      "v_exp_f32 v126, v126\n"
      "v_exp_f32 v127, v127\n"
      "v_cvt_pk_bf16_f32 v183, v124, v125\n"
      "v_exp_f32 v128, v128\n"
      "v_exp_f32 v129, v129\n"
      "v_cvt_pk_bf16_f32 v184, v126, v127\n"
      "v_cvt_pk_bf16_f32 v185, v128, v129\n"
      "v_exp_f32 v28, v28\n"
      "v_exp_f32 v29, v29\n"
      "v_mfma_f32_32x32x16_bf16 v[146:161], v[174:177], v[182:185], v[146:161]\n"
      "v_exp_f32 v30, v30\n"
      "v_exp_f32 v31, v31\n"
      "v_exp_f32 v32, v32\n"
      "v_cvt_pk_bf16_f32 v178, v28, v29\n"
      "v_exp_f32 v33, v33\n"
      "s_waitcnt lgkmcnt(2)\n"
      "v_mfma_f32_32x32x16_bf16 v[114:129], v[48:51], v[44:47], 0\n"
      "v_cvt_pk_bf16_f32 v179, v30, v31\n"
      "v_exp_f32 v34, v34\n"
      "v_exp_f32 v35, v35\n"
      "v_exp_f32 v36, v36\n"
      "v_cvt_pk_bf16_f32 v180, v32, v33\n"
      "v_exp_f32 v37, v37\n"
      "v_cvt_pk_bf16_f32 v181, v34, v35\n"
      "v_exp_f32 v38, v38\n"
      "v_exp_f32 v39, v39\n"
      "v_mfma_f32_32x32x16_bf16 v[130:145], v[170:173], v[178:181], v[130:145]\n"
      "v_cvt_pk_bf16_f32 v182, v36, v37\n"
      "v_exp_f32 v40, v40\n"
      "v_exp_f32 v41, v41\n"
      "v_cvt_pk_bf16_f32 v183, v38, v39\n"
      "v_exp_f32 v42, v42\n"
      "v_exp_f32 v43, v43\n"
      "v_cvt_pk_bf16_f32 v184, v40, v41\n"
      "v_cvt_pk_bf16_f32 v185, v42, v43\n"
      "v_exp_f32 v114, v114\n"
      "v_exp_f32 v115, v115\n"
      "v_mfma_f32_32x32x16_bf16 v[130:145], v[174:177], v[182:185], v[130:145]\n"
      "s_waitcnt lgkmcnt(0)\n"
      "ds_read_b128 v[52:55], v73 offset:32768\n"
      "ds_read_b128 v[170:173], v82 offset:0\n"
      "ds_read_b128 v[174:177], v82 offset:1024\n"
      "v_exp_f32 v116, v116\n"
      "v_exp_f32 v117, v117\n"
      "v_exp_f32 v118, v118\n"
      "v_cvt_pk_bf16_f32 v178, v114, v115\n"
      "v_exp_f32 v119, v119\n"
      "v_mfma_f32_32x32x16_bf16 v[28:43], v[48:51], v[8:11], 0\n"
      "v_cvt_pk_bf16_f32 v179, v116, v117\n"
      "v_exp_f32 v120, v120\n"
      "v_exp_f32 v121, v121\n"
      "v_exp_f32 v122, v122\n"
      "v_cvt_pk_bf16_f32 v180, v118, v119\n"
      "v_exp_f32 v123, v123\n"
      "v_cvt_pk_bf16_f32 v181, v120, v121\n"
      "v_exp_f32 v124, v124\n"
      "v_exp_f32 v125, v125\n"
      "v_mfma_f32_32x32x16_bf16 v[146:161], v[162:165], v[178:181], v[146:161]\n"
      "v_cvt_pk_bf16_f32 v182, v122, v123\n"
      "v_exp_f32 v126, v126\n"
      "v_exp_f32 v127, v127\n"
      "v_cvt_pk_bf16_f32 v183, v124, v125\n"
      "v_exp_f32 v128, v128\n"
      "v_exp_f32 v129, v129\n"
      "v_cvt_pk_bf16_f32 v184, v126, v127\n"
      "v_cvt_pk_bf16_f32 v185, v128, v129\n"
      "v_exp_f32 v28, v28\n"
      "v_exp_f32 v29, v29\n"
      "v_mfma_f32_32x32x16_bf16 v[146:161], v[166:169], v[182:185], v[146:161]\n"
      "v_exp_f32 v30, v30\n"
      "v_exp_f32 v31, v31\n"
      "v_exp_f32 v32, v32\n"
      "v_cvt_pk_bf16_f32 v178, v28, v29\n"
      "v_exp_f32 v33, v33\n"
      "s_waitcnt lgkmcnt(2)\n"
      "v_mfma_f32_32x32x16_bf16 v[114:129], v[52:55], v[44:47], 0\n"
      "v_cvt_pk_bf16_f32 v179, v30, v31\n"
      "v_exp_f32 v34, v34\n"
      "v_exp_f32 v35, v35\n"
      "v_exp_f32 v36, v36\n"
      "v_cvt_pk_bf16_f32 v180, v32, v33\n"
      "v_exp_f32 v37, v37\n"
      "v_cvt_pk_bf16_f32 v181, v34, v35\n"
      "v_exp_f32 v38, v38\n"
      "v_exp_f32 v39, v39\n"
      "v_mfma_f32_32x32x16_bf16 v[130:145], v[162:165], v[178:181], v[130:145]\n"
      "v_cvt_pk_bf16_f32 v182, v36, v37\n"
      "v_exp_f32 v40, v40\n"
      "v_exp_f32 v41, v41\n"
      "v_cvt_pk_bf16_f32 v183, v38, v39\n"
      "v_exp_f32 v42, v42\n"
      "v_exp_f32 v43, v43\n"
      "v_cvt_pk_bf16_f32 v184, v40, v41\n"
      "v_cvt_pk_bf16_f32 v185, v42, v43\n"
      "v_exp_f32 v114, v114\n"
      "v_exp_f32 v115, v115\n"
      "v_mfma_f32_32x32x16_bf16 v[130:145], v[166:169], v[182:185], v[130:145]\n"
      "s_waitcnt lgkmcnt(0)\n"
      "ds_read_b128 v[48:51], v73 offset:49152\n"
      "ds_read_b128 v[162:165], v84 offset:0\n"
      "ds_read_b128 v[166:169], v84 offset:1024\n"
      "v_exp_f32 v116, v116\n"
      "v_exp_f32 v117, v117\n"
      "v_exp_f32 v118, v118\n"
      "v_cvt_pk_bf16_f32 v178, v114, v115\n"
      "v_exp_f32 v119, v119\n"
      "v_mfma_f32_32x32x16_bf16 v[28:43], v[52:55], v[8:11], 0\n"
      "v_cvt_pk_bf16_f32 v179, v116, v117\n"
      "v_exp_f32 v120, v120\n"
      "v_exp_f32 v121, v121\n"
      "v_exp_f32 v122, v122\n"
      "v_cvt_pk_bf16_f32 v180, v118, v119\n"
      "v_exp_f32 v123, v123\n"
      "v_cvt_pk_bf16_f32 v181, v120, v121\n"
      "v_exp_f32 v124, v124\n"
      "v_exp_f32 v125, v125\n"
      "v_mfma_f32_32x32x16_bf16 v[146:161], v[170:173], v[178:181], v[146:161]\n"
      "v_cvt_pk_bf16_f32 v182, v122, v123\n"
      "v_exp_f32 v126, v126\n"
      "v_exp_f32 v127, v127\n"
      "v_cvt_pk_bf16_f32 v183, v124, v125\n"
      "v_exp_f32 v128, v128\n"
      "v_exp_f32 v129, v129\n"
      "v_cvt_pk_bf16_f32 v184, v126, v127\n"
      "v_cvt_pk_bf16_f32 v185, v128, v129\n"
      "v_exp_f32 v28, v28\n"
      "v_exp_f32 v29, v29\n"
      "v_mfma_f32_32x32x16_bf16 v[146:161], v[174:177], v[182:185], v[146:161]\n"
      "v_exp_f32 v30, v30\n"
      "v_exp_f32 v31, v31\n"
      "v_exp_f32 v32, v32\n"
      "v_cvt_pk_bf16_f32 v178, v28, v29\n"
      "v_exp_f32 v33, v33\n"
      "s_waitcnt lgkmcnt(2)\n"
      "v_mfma_f32_32x32x16_bf16 v[114:129], v[48:51], v[44:47], 0\n"
      "v_cvt_pk_bf16_f32 v179, v30, v31\n"
      "v_exp_f32 v34, v34\n"
      "v_exp_f32 v35, v35\n"
      "v_exp_f32 v36, v36\n"
      "v_cvt_pk_bf16_f32 v180, v32, v33\n"
      "v_exp_f32 v37, v37\n"
      "v_cvt_pk_bf16_f32 v181, v34, v35\n"
      "v_exp_f32 v38, v38\n"
      "v_exp_f32 v39, v39\n"
      "v_mfma_f32_32x32x16_bf16 v[130:145], v[170:173], v[178:181], v[130:145]\n"
      "v_cvt_pk_bf16_f32 v182, v36, v37\n"
      "v_exp_f32 v40, v40\n"
      "v_exp_f32 v41, v41\n"
      "v_cvt_pk_bf16_f32 v183, v38, v39\n"
      "v_exp_f32 v42, v42\n"
      "v_exp_f32 v43, v43\n"
      "v_cvt_pk_bf16_f32 v184, v40, v41\n"
      "v_cvt_pk_bf16_f32 v185, v42, v43\n"
      "v_exp_f32 v114, v114\n"
      "v_exp_f32 v115, v115\n"
      "v_mfma_f32_32x32x16_bf16 v[130:145], v[174:177], v[182:185], v[130:145]\n"
      "s_waitcnt lgkmcnt(0)\n"
      "ds_read_b128 v[52:55], v73 offset:0\n"
      "ds_read_b128 v[170:173], v78 offset:0\n"
      "ds_read_b128 v[174:177], v78 offset:1024\n"
      "v_exp_f32 v116, v116\n"
      "v_exp_f32 v117, v117\n"
      "v_exp_f32 v118, v118\n"
      "v_cvt_pk_bf16_f32 v178, v114, v115\n"
      "v_exp_f32 v119, v119\n"
      "v_mfma_f32_32x32x16_bf16 v[28:43], v[48:51], v[8:11], 0\n"
      "v_cvt_pk_bf16_f32 v179, v116, v117\n"
      "v_exp_f32 v120, v120\n"
      "v_exp_f32 v121, v121\n"
      "v_exp_f32 v122, v122\n"
      "v_cvt_pk_bf16_f32 v180, v118, v119\n"
      "v_exp_f32 v123, v123\n"
      "v_cvt_pk_bf16_f32 v181, v120, v121\n"
      "v_exp_f32 v124, v124\n"
      "v_exp_f32 v125, v125\n"
      "v_mfma_f32_32x32x16_bf16 v[146:161], v[162:165], v[178:181], v[146:161]\n"
      "v_cvt_pk_bf16_f32 v182, v122, v123\n"
      "v_exp_f32 v126, v126\n"
      "v_exp_f32 v127, v127\n"
      "v_cvt_pk_bf16_f32 v183, v124, v125\n"
      "v_exp_f32 v128, v128\n"
      "v_exp_f32 v129, v129\n"
      "v_cvt_pk_bf16_f32 v184, v126, v127\n"
      "v_cvt_pk_bf16_f32 v185, v128, v129\n"
      "v_exp_f32 v28, v28\n"
      "v_exp_f32 v29, v29\n"
      "v_mfma_f32_32x32x16_bf16 v[146:161], v[166:169], v[182:185], v[146:161]\n"
      "v_exp_f32 v30, v30\n"
      "v_exp_f32 v31, v31\n"
      "v_exp_f32 v32, v32\n"
      "v_cvt_pk_bf16_f32 v178, v28, v29\n"
      "v_exp_f32 v33, v33\n"
      "s_waitcnt lgkmcnt(2)\n"
      "v_mfma_f32_32x32x16_bf16 v[114:129], v[52:55], v[8:11], v[12:27]\n"
      "v_cvt_pk_bf16_f32 v179, v30, v31\n"
      "v_exp_f32 v34, v34\n"
      "v_exp_f32 v35, v35\n"
      "v_exp_f32 v36, v36\n"
      "v_cvt_pk_bf16_f32 v180, v32, v33\n"
      "v_exp_f32 v37, v37\n"
      "v_cvt_pk_bf16_f32 v181, v34, v35\n"
      "v_exp_f32 v38, v38\n"
      "v_exp_f32 v39, v39\n"
      "v_max3_f32 v186, |v146|, |v147|, |v148|\n"
      "v_max3_f32 v187, |v149|, |v150|, |v151|\n"
      "v_rcp_f32 v188, v154\n"
      "v_max3_f32 v186, v186, |v152|, |v153|\n"
      "v_cmp_nlt_f32 s[12:13], s33, v154\n"
      "v_max3_f32 v186, v186, v187, v154\n"
      "v_cmp_ngt_f32 vcc, s34, v186\n"
      "s_nop 1\n"
      "s_or_b64 vcc, s[12:13], vcc\n"
      "s_cbranch_scc1 .Lslow_2005_%=\n"
      ".Lback_2006_%=:\n"
      "v_mul_f32 v146, v146, v188\n"
      "v_mul_f32 v147, v147, v188\n"
      "v_mul_f32 v148, v148, v188\n"
      "v_mul_f32 v149, v149, v188\n"
      "v_mul_f32 v150, v150, v188\n"
      "v_mul_f32 v151, v151, v188\n"
      "v_mul_f32 v152, v152, v188\n"
      "v_mul_f32 v153, v153, v188\n"
      "v_cvt_pk_bf16_f32 v194, v146, v147\n"
      "v_cvt_pk_bf16_f32 v195, v148, v149\n"
      "v_cvt_pk_bf16_f32 v196, v150, v151\n"
      "v_cvt_pk_bf16_f32 v197, v152, v153\n"
      "buffer_store_dwordx4 v[194:197], v2, s[24:27], s70 offen sc1\n"
      "v_mfma_f32_32x32x16_bf16 v[130:145], v[162:165], v[178:181], v[130:145]\n"
      "v_cvt_pk_bf16_f32 v182, v36, v37\n"
      "v_exp_f32 v40, v40\n"
      "v_exp_f32 v41, v41\n"
      "v_cvt_pk_bf16_f32 v183, v38, v39\n"
      "v_exp_f32 v42, v42\n"
      "v_exp_f32 v43, v43\n"
      "v_cvt_pk_bf16_f32 v184, v40, v41\n"
      "v_cvt_pk_bf16_f32 v185, v42, v43\n"
      "v_exp_f32 v114, v114\n"
      "v_exp_f32 v115, v115\n"
      "v_mfma_f32_32x32x16_bf16 v[130:145], v[166:169], v[182:185], v[130:145]\n"
      "v_exp_f32 v116, v116\n"
      "v_exp_f32 v117, v117\n"
      "v_exp_f32 v118, v118\n"
      "v_cvt_pk_bf16_f32 v178, v114, v115\n"
      "v_exp_f32 v119, v119\n"
      "v_cvt_pk_bf16_f32 v179, v116, v117\n"
      "v_exp_f32 v120, v120\n"
      "v_exp_f32 v121, v121\n"
      "v_exp_f32 v122, v122\n"
      "v_cvt_pk_bf16_f32 v180, v118, v119\n"
      "v_exp_f32 v123, v123\n"
      "v_cvt_pk_bf16_f32 v181, v120, v121\n"
      "v_exp_f32 v124, v124\n"
      "v_exp_f32 v125, v125\n"
      "s_waitcnt lgkmcnt(1)\n"
      "v_mfma_f32_32x32x16_bf16 v[130:145], v[170:173], v[178:181], v[130:145]\n"
      "v_cvt_pk_bf16_f32 v182, v122, v123\n"
      "v_exp_f32 v126, v126\n"
      "v_exp_f32 v127, v127\n"
      "v_cvt_pk_bf16_f32 v183, v124, v125\n"
      "v_exp_f32 v128, v128\n"
      "v_exp_f32 v129, v129\n"
      "v_cvt_pk_bf16_f32 v184, v126, v127\n"
      "v_cvt_pk_bf16_f32 v185, v128, v129\n"
      "s_waitcnt lgkmcnt(0)\n"
      "s_nop 0\n"
      "v_mfma_f32_32x32x16_bf16 v[130:145], v[174:177], v[182:185], v[130:145]\n"
      "s_nop 11\n"
      "v_max3_f32 v186, |v130|, |v131|, |v132|\n"
      "v_max3_f32 v187, |v133|, |v134|, |v135|\n"
      "v_rcp_f32 v188, v138\n"
      "v_max3_f32 v186, v186, |v136|, |v137|\n"
      "v_cmp_nlt_f32 s[12:13], s33, v138\n"
      "v_max3_f32 v186, v186, v187, v138\n"
      "v_cmp_ngt_f32 vcc, s34, v186\n"
      "s_nop 1\n"
      "s_or_b64 vcc, s[12:13], vcc\n"
      "s_cbranch_scc1 .Lslow_2007_%=\n"
      ".Lback_2008_%=:\n"
      "v_mul_f32 v130, v130, v188\n"
      "v_mul_f32 v131, v131, v188\n"
      "v_mul_f32 v132, v132, v188\n"
      "v_mul_f32 v133, v133, v188\n"
      "v_mul_f32 v134, v134, v188\n"
      "v_mul_f32 v135, v135, v188\n"
      "v_mul_f32 v136, v136, v188\n"
      "v_mul_f32 v137, v137, v188\n"
      "v_cvt_pk_bf16_f32 v190, v130, v131\n"
      "v_cvt_pk_bf16_f32 v191, v132, v133\n"
      "v_cvt_pk_bf16_f32 v192, v134, v135\n"
      "v_cvt_pk_bf16_f32 v193, v136, v137\n"
      "buffer_store_dwordx4 v[190:193], v2, s[24:27], s69 offen sc1\n"
      "s_barrier\n"
      "v_and_b32 v9, 0xffff0000, v106\n"
      "v_lshlrev_b32 v8, 16, v106\n"
      "v_and_b32 v11, 0xffff0000, v107\n"
      "v_lshlrev_b32 v10, 16, v107\n"
      "v_and_b32 v13, 0xffff0000, v108\n"
      "v_lshlrev_b32 v12, 16, v108\n"
      "v_and_b32 v15, 0xffff0000, v109\n"
      "v_lshlrev_b32 v14, 16, v109\n"
      "v_and_b32 v17, 0xffff0000, v110\n"
      "v_lshlrev_b32 v16, 16, v110\n"
      "v_and_b32 v19, 0xffff0000, v111\n"
      "v_lshlrev_b32 v18, 16, v111\n"
      "v_and_b32 v21, 0xffff0000, v112\n"
      "v_lshlrev_b32 v20, 16, v112\n"
      "v_and_b32 v23, 0xffff0000, v113\n"
      "v_lshlrev_b32 v22, 16, v113\n"
      "v_lshl_add_u32 v92, v6, 9, v92\n"
      "s_lshl_b32 s52, s50, 15\n"
      "v_add_u32 v7, s52, v92\n"
      "v_xor_b32 v40, 64, v7\n"
      "s_lshl_b32 s52, s38, 12\n"
      "s_add_u32 s52, s52, 147456\n"
      "v_add_u32 v41, s52, v2\n"
      "v_xor_b32 v42, 0x2000, v41\n"
      "s_lshl_b32 s62, s75, 15\n"
      "s_lshl_b32 s59, s40, 12\n"
      "s_add_u32 s62, s62, s59\n"
      "s_lshl_b32 s59, s50, 11\n"
      "s_add_u32 s62, s62, s59\n"
      "s_lshr_b32 s63, s62, 3\n"
      "s_add_u32 s63, s63, 524288\n"
      "s_and_b32 s59, s63, 0xffffe000\n"
      "s_add_u32 s63, s63, s59\n"
      "s_add_u32 s63, s63, s76\n"
      "s_add_u32 s62, s62, 393216\n"
      "s_and_b32 s59, s62, 0xffffe000\n"
      "s_add_u32 s62, s62, s59\n"
      "s_add_u32 s62, s62, s76\n"
      "s_lshl_b32 s20, s75, 16\n"
      "s_lshl_b32 s59, s50, 15\n"
      "s_add_u32 s20, s20, s59\n"
      "s_add_u32 s20, s20, s76\n"
      "s_add_u32 s20, s20, 524288\n"
      "s_add_u32 s21, s20, 4096\n"
      "s_add_u32 s22, s20, 16384\n"
      "s_add_u32 s23, s20, 20480\n"
      "s_lshl_b32 s52, s44, 10\n"
      "s_lshl_b32 s59, s38, 8\n"
      "s_add_u32 s52, s52, s59\n"
      "s_add_u32 s52, s52, 950272\n"
      "s_and_b32 s59, s52, 0xffffe000\n"
      "s_add_u32 s52, s52, s59\n"
      "s_add_u32 s52, s52, s76\n"
      "s_add_u32 s10, s24, s52\n"
      "s_addc_u32 s11, s25, 0\n"
      "v_lshl_add_u32 v43, v6, 2, v91\n"
      "v_mul_u32_u24 v43, 192, v43\n"
      "v_add3_u32 v43, v43, v90, 0\n"
      "v_add_u32 v43, 49152, v43\n"
      "v_add_u32 v44, 49152, v43\n"
      "s_lshl_b32 s64, s38, 16\n"
      "s_add_u32 s64, s64, s76\n"
      "s_add_u32 s64, s64, 786432\n"
      "s_add_u32 s65, s64, 4096\n"
      "s_add_u32 s66, s64, 16384\n"
      "s_add_u32 s67, s64, 20480\n"
      "s_add_u32 s68, s64, 32768\n"
      "s_add_u32 s69, s64, 36864\n"
      "s_add_u32 s70, s64, 49152\n"
      "s_add_u32 s71, s64, 53248\n"
      "s_mul_i32 s53, s38, 196608\n"
      "s_add_u32 s53, s53, s76\n"
      "s_add_u32 s53, s53, 1114112\n"
      "s_lshl_b32 s55, s45, 11\n"
      "s_cmp_ge_u32 s45, 22\n"
      "s_cselect_b32 s4, 5120, 0\n"
      "s_add_u32 s4, s4, s55\n"
      "s_and_b32 s59, s4, 0xffffe000\n"
      "s_add_u32 s4, s4, s59\n"
      "s_add_u32 s4, s4, s53\n"
      "s_cmp_ge_u32 s45, 21\n"
      "s_cselect_b32 s5, 5120, 0\n"
      "s_add_u32 s5, s5, 1024\n"
      "s_add_u32 s5, s5, s55\n"
      "s_and_b32 s59, s5, 0xffffe000\n"
      "s_add_u32 s5, s5, s59\n"
      "s_add_u32 s5, s5, s53\n"
      "s_cmp_ge_u32 s45, 21\n"
      "s_cselect_b32 s6, 5120, 0\n"
      "s_add_u32 s6, s6, 2048\n"
      "s_waitcnt vmcnt(0)\n"
      "s_barrier\n"
      "buffer_store_dwordx4 v[96:99], v101, s[24:27], s72 offen sc1\n"
      "s_add_u32 s6, s6, s55\n"
      "s_and_b32 s59, s6, 0xffffe000\n"
      "s_add_u32 s6, s6, s59\n"
      "s_add_u32 s6, s6, s53\n"
      "s_cmp_ge_u32 s45, 20\n"
      "s_cselect_b32 s7, 5120, 0\n"
      "s_add_u32 s7, s7, 3072\n"
      "s_add_u32 s7, s7, s55\n"
      "s_and_b32 s59, s7, 0xffffe000\n"
      "s_add_u32 s7, s7, s59\n"
      "s_add_u32 s7, s7, s53\n"
      "s_cmp_ge_u32 s45, 20\n"
      "s_cselect_b32 s8, 5120, 0\n"
      "s_add_u32 s8, s8, 4096\n"
      "s_add_u32 s8, s8, s55\n"
      "s_and_b32 s59, s8, 0xffffe000\n"
      "s_add_u32 s8, s8, s59\n"
      "s_add_u32 s8, s8, s53\n"
      "s_cmp_ge_u32 s45, 19\n"
      "s_cselect_b32 s9, 5120, 0\n"
      "ds_read_b64_tr_b16 v[56:57], v40 offset:0\n"
      "ds_read_b64_tr_b16 v[58:59], v40 offset:1024\n"
      "ds_read_b64_tr_b16 v[60:61], v7 offset:0\n"
      "ds_read_b64_tr_b16 v[62:63], v7 offset:1024\n"
      "ds_read_b64_tr_b16 v[64:65], v40 offset:2048\n"
      "ds_read_b64_tr_b16 v[66:67], v40 offset:3072\n"
      "ds_read_b64_tr_b16 v[68:69], v7 offset:2048\n"
      "ds_read_b64_tr_b16 v[70:71], v7 offset:3072\n"
      "s_sleep 3\n"
      "s_mov_b32 s60, 0x100001\n"
      "s_load_dwordx2 s[12:13], s[10:11], 0x0 glc\n"
      "s_load_dwordx2 s[14:15], s[10:11], 0x80 glc\n"
      "s_add_u32 s9, s9, 5120\n"
      "s_add_u32 s9, s9, s55\n"
      "s_and_b32 s59, s9, 0xffffe000\n"
      "s_add_u32 s9, s9, s59\n"
      "s_add_u32 s9, s9, s53\n"
      "s_cmp_eq_u32 s46, 3\n"
      "s_cselect_b32 s52, 0, 1073741824\n"
      "v_add_u32 v45, s52, v2\n"
      "v_add_u32 v46, 147456, v2\n"
      "s_lshl_b32 s52, s38, 10\n"
      "v_add_u32 v47, s52, v46\n"
      "s_lshl_b32 s52, s75, 7\n"
      "s_add_u32 s52, s52, 155648\n"
      "v_lshl_add_u32 v48, v5, 2, s52\n"
      "s_min_u32 s53, s38, 2\n"
      "s_lshl_b32 s53, s53, 6\n"
      "v_add_u32 v49, s53, v43\n"
      "v_add_u32 v54, s53, v44\n"
      "s_lshl_b32 s53, s38, 10\n"
      "s_add_u32 s53, s53, 147456\n"
      "v_lshl_add_u32 v55, v5, 4, s53\n"
      "v_lshrrev_b32 v50, 2, v1\n"
      "v_xor_b32 v50, v50, v1\n"
      "v_and_b32 v50, 4, v50\n"
      "v_cmp_eq_u32 vcc, 0, v50\n"
      "v_mov_b32 v51, 0x3f803f80\n"
      "s_nop 0\n"
      "v_cndmask_b32 v50, 0, v51, vcc\n"
      "v_mov_b32 v51, v50\n"
      "v_mov_b32 v52, v50\n"
      "v_mov_b32 v53, v50\n"
      "s_branch .Lscheck_2010_%=\n"
      "s_nop 0\n"
      ".Lspoll_2009_%=:\n"
      "s_load_dwordx2 s[12:13], s[10:11], 0x0 glc\n"
      "s_load_dwordx2 s[14:15], s[10:11], 0x80 glc\n"
      ".Lscheck_2010_%=:\n"
      "s_sub_u32 s60, s60, 1\n"
      "s_waitcnt lgkmcnt(0)\n"
      "s_cbranch_scc1 .Lspollend_2011_%=\n"
      "s_cmp_eq_u64 s[12:13], s[36:37]\n"
      "s_cbranch_scc0 .Lspoll_2009_%=\n"
      "s_cmp_eq_u64 s[14:15], s[36:37]\n"
      "s_cbranch_scc0 .Lspoll_2009_%=\n"
      ".Lspollend_2011_%=:\n"
      "s_barrier\n"
      "buffer_load_dwordx4 v[76:79], v2, s[24:27], s20 offen sc1\n"
      "buffer_load_dwordx4 v[80:83], v2, s[24:27], s20 offen offset:1024 sc1\n"
      "buffer_load_dwordx4 v[84:87], v2, s[24:27], s20 offen offset:2048 sc1\n"
      "buffer_load_dwordx4 v[88:91], v2, s[24:27], s20 offen offset:3072 sc1\n"
      "buffer_load_dwordx4 v[104:107], v2, s[24:27], s21 offen sc1\n"
      "buffer_load_dwordx4 v[108:111], v2, s[24:27], s21 offen offset:1024 sc1\n"
      "s_waitcnt vmcnt(5)\n"
      "v_mfma_f32_32x32x16_bf16 v[24:39], v[56:59], v[76:79], 0\n"
      "ds_read_b64_tr_b16 v[72:73], v40 offset:4096\n"
      "ds_read_b64_tr_b16 v[74:75], v40 offset:5120\n"
      "v_mfma_f32_32x32x16_bf16 v[8:23], v[60:63], v[76:79], v[8:23]\n"
      "ds_read_b64_tr_b16 v[56:57], v7 offset:4096\n"
      "ds_read_b64_tr_b16 v[58:59], v7 offset:5120\n"
      "buffer_load_dwordx4 v[76:79], v2, s[24:27], s21 offen offset:2048 sc1\n"
      "s_waitcnt vmcnt(5)\n"
      "v_mfma_f32_32x32x16_bf16 v[24:39], v[64:67], v[80:83], v[24:39]\n"
      "ds_read_b64_tr_b16 v[60:61], v40 offset:6144\n"
      "ds_read_b64_tr_b16 v[62:63], v40 offset:7168\n"
      "v_mfma_f32_32x32x16_bf16 v[8:23], v[68:71], v[80:83], v[8:23]\n"
      "ds_read_b64_tr_b16 v[64:65], v7 offset:6144\n"
      "ds_read_b64_tr_b16 v[66:67], v7 offset:7168\n"
      "buffer_load_dwordx4 v[80:83], v2, s[24:27], s21 offen offset:3072 sc1\n"
      "s_waitcnt vmcnt(5) lgkmcnt(6)\n"
      "v_mfma_f32_32x32x16_bf16 v[24:39], v[72:75], v[84:87], v[24:39]\n"
      "ds_read_b64_tr_b16 v[68:69], v40 offset:8192\n"
      "ds_read_b64_tr_b16 v[70:71], v40 offset:9216\n"
      "s_waitcnt lgkmcnt(6)\n"
      "v_mfma_f32_32x32x16_bf16 v[8:23], v[56:59], v[84:87], v[8:23]\n"
      "ds_read_b64_tr_b16 v[72:73], v7 offset:8192\n"
      "ds_read_b64_tr_b16 v[74:75], v7 offset:9216\n"
      "buffer_load_dwordx4 v[84:87], v2, s[24:27], s22 offen sc1\n"
      "s_waitcnt vmcnt(5) lgkmcnt(6)\n"
      "v_mfma_f32_32x32x16_bf16 v[24:39], v[60:63], v[88:91], v[24:39]\n"
      "ds_read_b64_tr_b16 v[56:57], v40 offset:10240\n"
      "ds_read_b64_tr_b16 v[58:59], v40 offset:11264\n"
      "s_waitcnt lgkmcnt(6)\n"
      "v_mfma_f32_32x32x16_bf16 v[8:23], v[64:67], v[88:91], v[8:23]\n"
      "ds_read_b64_tr_b16 v[60:61], v7 offset:10240\n"
      "ds_read_b64_tr_b16 v[62:63], v7 offset:11264\n"
      "buffer_load_dwordx4 v[88:91], v2, s[24:27], s22 offen offset:1024 sc1\n"
      "s_waitcnt vmcnt(5) lgkmcnt(6)\n"
      "v_mfma_f32_32x32x16_bf16 v[24:39], v[68:71], v[104:107], v[24:39]\n"
      "ds_read_b64_tr_b16 v[64:65], v40 offset:12288\n"
      "ds_read_b64_tr_b16 v[66:67], v40 offset:13312\n"
      "s_waitcnt lgkmcnt(6)\n"
      "v_mfma_f32_32x32x16_bf16 v[8:23], v[72:75], v[104:107], v[8:23]\n"
      "ds_read_b64_tr_b16 v[68:69], v7 offset:12288\n"
      "ds_read_b64_tr_b16 v[70:71], v7 offset:13312\n"
      "buffer_load_dwordx4 v[104:107], v2, s[24:27], s22 offen offset:2048 sc1\n"
      "s_waitcnt vmcnt(5) lgkmcnt(6)\n"
      "v_mfma_f32_32x32x16_bf16 v[24:39], v[56:59], v[108:111], v[24:39]\n"
      "ds_read_b64_tr_b16 v[72:73], v40 offset:14336\n"
      "ds_read_b64_tr_b16 v[74:75], v40 offset:15360\n"
      "s_waitcnt lgkmcnt(6)\n"
      "v_mfma_f32_32x32x16_bf16 v[8:23], v[60:63], v[108:111], v[8:23]\n"
      "ds_read_b64_tr_b16 v[56:57], v7 offset:14336\n"
      "ds_read_b64_tr_b16 v[58:59], v7 offset:15360\n"
      "buffer_load_dwordx4 v[108:111], v2, s[24:27], s22 offen offset:3072 sc1\n"
      "s_waitcnt vmcnt(5) lgkmcnt(6)\n"
      "v_mfma_f32_32x32x16_bf16 v[24:39], v[64:67], v[76:79], v[24:39]\n"
      "ds_read_b64_tr_b16 v[60:61], v40 offset:16384\n"
      "ds_read_b64_tr_b16 v[62:63], v40 offset:17408\n"
      "s_waitcnt lgkmcnt(6)\n"
      "v_mfma_f32_32x32x16_bf16 v[8:23], v[68:71], v[76:79], v[8:23]\n"
      "ds_read_b64_tr_b16 v[64:65], v7 offset:16384\n"
      "ds_read_b64_tr_b16 v[66:67], v7 offset:17408\n"
      "buffer_load_dwordx4 v[76:79], v2, s[24:27], s23 offen sc1\n"
      "s_waitcnt vmcnt(5) lgkmcnt(6)\n"
      "v_mfma_f32_32x32x16_bf16 v[24:39], v[72:75], v[80:83], v[24:39]\n"
      "ds_read_b64_tr_b16 v[68:69], v40 offset:18432\n"
      "ds_read_b64_tr_b16 v[70:71], v40 offset:19456\n"
      "s_waitcnt lgkmcnt(6)\n"
      "v_mfma_f32_32x32x16_bf16 v[8:23], v[56:59], v[80:83], v[8:23]\n"
      "ds_read_b64_tr_b16 v[72:73], v7 offset:18432\n"
      "ds_read_b64_tr_b16 v[74:75], v7 offset:19456\n"
      "buffer_load_dwordx4 v[80:83], v2, s[24:27], s23 offen offset:1024 sc1\n"
      "s_waitcnt vmcnt(5) lgkmcnt(6)\n"
      "v_mfma_f32_32x32x16_bf16 v[24:39], v[60:63], v[84:87], v[24:39]\n"
      "ds_read_b64_tr_b16 v[56:57], v40 offset:20480\n"
      "ds_read_b64_tr_b16 v[58:59], v40 offset:21504\n"
      "s_waitcnt lgkmcnt(6)\n"
      "v_mfma_f32_32x32x16_bf16 v[8:23], v[64:67], v[84:87], v[8:23]\n"
      "ds_read_b64_tr_b16 v[60:61], v7 offset:20480\n"
      "ds_read_b64_tr_b16 v[62:63], v7 offset:21504\n"
      "buffer_load_dwordx4 v[84:87], v2, s[24:27], s23 offen offset:2048 sc1\n"
      "s_waitcnt vmcnt(5) lgkmcnt(6)\n"
      "v_mfma_f32_32x32x16_bf16 v[24:39], v[68:71], v[88:91], v[24:39]\n"
      "ds_read_b64_tr_b16 v[64:65], v40 offset:22528\n"
      "ds_read_b64_tr_b16 v[66:67], v40 offset:23552\n"
      "s_waitcnt lgkmcnt(6)\n"
      "v_mfma_f32_32x32x16_bf16 v[8:23], v[72:75], v[88:91], v[8:23]\n"
      "ds_read_b64_tr_b16 v[68:69], v7 offset:22528\n"
      "ds_read_b64_tr_b16 v[70:71], v7 offset:23552\n"
      "buffer_load_dwordx4 v[88:91], v2, s[24:27], s23 offen offset:3072 sc1\n"
      "s_barrier\n"
      "s_waitcnt vmcnt(5) lgkmcnt(6)\n"
      "v_mfma_f32_32x32x16_bf16 v[24:39], v[56:59], v[104:107], v[24:39]\n"
      "ds_read_b64_tr_b16 v[72:73], v40 offset:24576\n"
      "ds_read_b64_tr_b16 v[74:75], v40 offset:25600\n"
      "s_waitcnt lgkmcnt(6)\n"
      "v_mfma_f32_32x32x16_bf16 v[8:23], v[60:63], v[104:107], v[8:23]\n"
      "ds_read_b64_tr_b16 v[56:57], v7 offset:24576\n"
      "ds_read_b64_tr_b16 v[58:59], v7 offset:25600\n"
      "s_waitcnt vmcnt(4) lgkmcnt(6)\n"
      "v_mfma_f32_32x32x16_bf16 v[24:39], v[64:67], v[108:111], v[24:39]\n"
      "ds_read_b64_tr_b16 v[60:61], v40 offset:26624\n"
      "ds_read_b64_tr_b16 v[62:63], v40 offset:27648\n"
      "s_waitcnt lgkmcnt(6)\n"
      "v_mfma_f32_32x32x16_bf16 v[8:23], v[68:71], v[108:111], v[8:23]\n"
      "ds_read_b64_tr_b16 v[64:65], v7 offset:26624\n"
      "ds_read_b64_tr_b16 v[66:67], v7 offset:27648\n"
      "s_waitcnt vmcnt(3) lgkmcnt(6)\n"
      "v_mfma_f32_32x32x16_bf16 v[24:39], v[72:75], v[76:79], v[24:39]\n"
      "ds_read_b64_tr_b16 v[68:69], v40 offset:28672\n"
      "ds_read_b64_tr_b16 v[70:71], v40 offset:29696\n"
      "s_waitcnt lgkmcnt(6)\n"
      "v_mfma_f32_32x32x16_bf16 v[8:23], v[56:59], v[76:79], v[8:23]\n"
      "ds_read_b64_tr_b16 v[72:73], v7 offset:28672\n"
      "ds_read_b64_tr_b16 v[74:75], v7 offset:29696\n"
      "s_waitcnt vmcnt(2) lgkmcnt(6)\n"
      "v_mfma_f32_32x32x16_bf16 v[24:39], v[60:63], v[80:83], v[24:39]\n"
      "ds_read_b64_tr_b16 v[56:57], v40 offset:30720\n"
      "ds_read_b64_tr_b16 v[58:59], v40 offset:31744\n"
      "s_waitcnt lgkmcnt(6)\n"
      "v_mfma_f32_32x32x16_bf16 v[8:23], v[64:67], v[80:83], v[8:23]\n"
      "ds_read_b64_tr_b16 v[60:61], v7 offset:30720\n"
      "ds_read_b64_tr_b16 v[62:63], v7 offset:31744\n"
      "s_waitcnt vmcnt(1) lgkmcnt(6)\n"
      "v_mfma_f32_32x32x16_bf16 v[24:39], v[68:71], v[84:87], v[24:39]\n"
      "s_waitcnt lgkmcnt(4)\n"
      "v_mfma_f32_32x32x16_bf16 v[8:23], v[72:75], v[84:87], v[8:23]\n"
      "s_waitcnt vmcnt(0) lgkmcnt(2)\n"
      "v_mfma_f32_32x32x16_bf16 v[24:39], v[56:59], v[88:91], v[24:39]\n"
      "s_waitcnt lgkmcnt(0)\n"
      "v_mfma_f32_32x32x16_bf16 v[8:23], v[60:63], v[88:91], v[8:23]\n"
      "s_nop 9\n"
      "ds_write_b128 v41, v[24:27] offset:0\n"
      "ds_write_b128 v41, v[28:31] offset:1024\n"
      "ds_write_b128 v41, v[32:35] offset:2048\n"
      "ds_write_b128 v41, v[36:39] offset:3072\n"
      "s_waitcnt lgkmcnt(0)\n"
      "s_barrier\n"
      "ds_read_b128 v[24:27], v42 offset:0\n"
      "ds_read_b128 v[28:31], v42 offset:1024\n"
      "ds_read_b128 v[32:35], v42 offset:2048\n"
      "ds_read_b128 v[36:39], v42 offset:3072\n"
      "s_waitcnt lgkmcnt(3)\n"
      "v_pk_add_f32 v[8:9], v[8:9], v[24:25]\n"
      "v_pk_add_f32 v[10:11], v[10:11], v[26:27]\n"
      "s_waitcnt lgkmcnt(2)\n"
      "v_pk_add_f32 v[12:13], v[12:13], v[28:29]\n"
      "v_pk_add_f32 v[14:15], v[14:15], v[30:31]\n"
      "v_cvt_pk_bf16_f32 v56, v8, v9\n"
      "v_cvt_pk_bf16_f32 v57, v10, v11\n"
      "v_cvt_pk_bf16_f32 v58, v12, v13\n"
      "v_cvt_pk_bf16_f32 v59, v14, v15\n"
      "buffer_store_dwordx4 v[56:59], v2, s[24:27], s62 offen sc1\n"
      "s_waitcnt lgkmcnt(1)\n"
      "v_pk_add_f32 v[16:17], v[16:17], v[32:33]\n"
      "v_pk_add_f32 v[18:19], v[18:19], v[34:35]\n"
      "s_waitcnt lgkmcnt(0)\n"
      "v_pk_add_f32 v[20:21], v[20:21], v[36:37]\n"
      "v_pk_add_f32 v[22:23], v[22:23], v[38:39]\n"
      "v_cvt_pk_bf16_f32 v60, v16, v17\n"
      "v_cvt_pk_bf16_f32 v61, v18, v19\n"
      "v_cvt_pk_bf16_f32 v62, v20, v21\n"
      "v_cvt_pk_bf16_f32 v63, v22, v23\n"
      "buffer_store_dwordx4 v[60:63], v2, s[24:27], s62 offen offset:1024 sc1\n"
      "v_pk_add_f32 v[24:25], v[8:9], v[10:11]\n"
      "v_pk_mul_f32 v[26:27], v[8:9], v[8:9]\n"
      "s_nop 0\n"
      "v_pk_fma_f32 v[26:27], v[10:11], v[10:11], v[26:27]\n"
      "v_pk_add_f32 v[24:25], v[24:25], v[12:13]\n"
      "v_pk_fma_f32 v[26:27], v[12:13], v[12:13], v[26:27]\n"
      "v_pk_add_f32 v[24:25], v[24:25], v[14:15]\n"
      "v_pk_fma_f32 v[26:27], v[14:15], v[14:15], v[26:27]\n"
      "v_pk_add_f32 v[24:25], v[24:25], v[16:17]\n"
      "v_pk_fma_f32 v[26:27], v[16:17], v[16:17], v[26:27]\n"
      ".Lpage6_%=:\n"
      "s_cbranch_execz .Lret_%=\n"
      "v_pk_add_f32 v[24:25], v[24:25], v[18:19]\n"
      "v_pk_fma_f32 v[26:27], v[18:19], v[18:19], v[26:27]\n"
      "v_pk_add_f32 v[24:25], v[24:25], v[20:21]\n"
      "v_pk_fma_f32 v[26:27], v[20:21], v[20:21], v[26:27]\n"
      "v_pk_add_f32 v[24:25], v[24:25], v[22:23]\n"
      "v_pk_fma_f32 v[26:27], v[22:23], v[22:23], v[26:27]\n"
      "v_add_f32 v24, v24, v25\n"
      "v_add_f32 v26, v26, v27\n"
      "v_lshlrev_b32 v28, 3, v5\n"
      "v_lshl_add_u32 v28, v6, 2, v28\n"
      "v_permlane32_swap_b32 v24, v26\n"
      "v_add_f32 v24, v24, v26\n"
      "buffer_store_dword v24, v28, s[24:27], s63 offen sc1\n"
      "s_barrier\n"
      ".LphaseD_%=:\n"
      "ds_read_b64_tr_b16 v[104:105], v49 offset:0\n"
      "ds_read_b64_tr_b16 v[106:107], v49 offset:1536\n"
      "ds_read_b64_tr_b16 v[108:109], v49 offset:3072\n"
      "ds_read_b64_tr_b16 v[110:111], v49 offset:4608\n"
      "ds_read_b64_tr_b16 v[112:113], v49 offset:6144\n"
      "ds_read_b64_tr_b16 v[114:115], v49 offset:7680\n"
      "ds_read_b64_tr_b16 v[116:117], v49 offset:9216\n"
      "ds_read_b64_tr_b16 v[118:119], v49 offset:10752\n"
      "ds_read_b64_tr_b16 v[120:121], v49 offset:12288\n"
      "ds_read_b64_tr_b16 v[122:123], v49 offset:13824\n"
      "ds_read_b64_tr_b16 v[124:125], v49 offset:15360\n"
      "ds_read_b64_tr_b16 v[126:127], v49 offset:16896\n"
      "s_lshl_b32 s52, s44, 11\n"
      "s_lshl_b32 s59, s38, 9\n"
      "s_add_u32 s52, s52, s59\n"
      "s_add_u32 s52, s52, 966656\n"
      "s_and_b32 s59, s52, 0xffffe000\n"
      "s_add_u32 s52, s52, s59\n"
      "s_add_u32 s52, s52, s76\n"
      "s_add_u32 s10, s24, s52\n"
      "s_addc_u32 s11, s25, 0\n"
      "s_waitcnt lgkmcnt(8)\n"
      "v_mfma_f32_16x16x32_bf16 v[76:79], v[50:53], v[104:107], 0\n"
      "ds_read_b64_tr_b16 v[128:129], v49 offset:18432\n"
      "ds_read_b64_tr_b16 v[130:131], v49 offset:19968\n"
      "v_mfma_f32_16x16x32_bf16 v[76:79], v[50:53], v[108:111], v[76:79]\n"
      "ds_read_b64_tr_b16 v[132:133], v49 offset:21504\n"
      "ds_read_b64_tr_b16 v[134:135], v49 offset:23040\n"
      "s_waitcnt lgkmcnt(8)\n"
      "v_mfma_f32_16x16x32_bf16 v[76:79], v[50:53], v[112:115], v[76:79]\n"
      "ds_read_b64_tr_b16 v[104:105], v49 offset:24576\n"
      "ds_read_b64_tr_b16 v[106:107], v49 offset:26112\n"
      "v_mfma_f32_16x16x32_bf16 v[76:79], v[50:53], v[116:119], v[76:79]\n"
      "ds_read_b64_tr_b16 v[108:109], v49 offset:27648\n"
      "ds_read_b64_tr_b16 v[110:111], v49 offset:29184\n"
      "s_waitcnt lgkmcnt(8)\n"
      "v_mfma_f32_16x16x32_bf16 v[76:79], v[50:53], v[120:123], v[76:79]\n"
      "ds_read_b64_tr_b16 v[112:113], v49 offset:30720\n"
      "ds_read_b64_tr_b16 v[114:115], v49 offset:32256\n"
      "v_mfma_f32_16x16x32_bf16 v[76:79], v[50:53], v[124:127], v[76:79]\n"
      "ds_read_b64_tr_b16 v[116:117], v49 offset:33792\n"
      "ds_read_b64_tr_b16 v[118:119], v49 offset:35328\n"
      "s_waitcnt lgkmcnt(8)\n"
      "v_mfma_f32_16x16x32_bf16 v[76:79], v[50:53], v[128:131], v[76:79]\n"
      "ds_read_b64_tr_b16 v[120:121], v49 offset:36864\n"
      "ds_read_b64_tr_b16 v[122:123], v49 offset:38400\n"
      "v_mfma_f32_16x16x32_bf16 v[76:79], v[50:53], v[132:135], v[76:79]\n"
      "ds_read_b64_tr_b16 v[124:125], v49 offset:39936\n"
      "ds_read_b64_tr_b16 v[126:127], v49 offset:41472\n"
      "s_waitcnt lgkmcnt(8)\n"
      "v_mfma_f32_16x16x32_bf16 v[76:79], v[50:53], v[104:107], v[76:79]\n"
      "ds_read_b64_tr_b16 v[128:129], v49 offset:43008\n"
      "ds_read_b64_tr_b16 v[130:131], v49 offset:44544\n"
      "v_mfma_f32_16x16x32_bf16 v[76:79], v[50:53], v[108:111], v[76:79]\n"
      "ds_read_b64_tr_b16 v[132:133], v49 offset:46080\n"
      "ds_read_b64_tr_b16 v[134:135], v49 offset:47616\n"
      "s_waitcnt lgkmcnt(8)\n"
      "v_mfma_f32_16x16x32_bf16 v[76:79], v[50:53], v[112:115], v[76:79]\n"
      "ds_read_b64_tr_b16 v[104:105], v54 offset:0\n"
      "ds_read_b64_tr_b16 v[106:107], v54 offset:1536\n"
      "v_mfma_f32_16x16x32_bf16 v[76:79], v[50:53], v[116:119], v[76:79]\n"
      "ds_read_b64_tr_b16 v[108:109], v54 offset:3072\n"
      "ds_read_b64_tr_b16 v[110:111], v54 offset:4608\n"
      "s_waitcnt lgkmcnt(8)\n"
      "v_mfma_f32_16x16x32_bf16 v[76:79], v[50:53], v[120:123], v[76:79]\n"
      "ds_read_b64_tr_b16 v[112:113], v54 offset:6144\n"
      "ds_read_b64_tr_b16 v[114:115], v54 offset:7680\n"
      "v_mfma_f32_16x16x32_bf16 v[76:79], v[50:53], v[124:127], v[76:79]\n"
      "ds_read_b64_tr_b16 v[116:117], v54 offset:9216\n"
      "ds_read_b64_tr_b16 v[118:119], v54 offset:10752\n"
      "s_waitcnt lgkmcnt(8)\n"
      "v_mfma_f32_16x16x32_bf16 v[76:79], v[50:53], v[128:131], v[76:79]\n"
      "ds_read_b64_tr_b16 v[120:121], v54 offset:12288\n"
      "ds_read_b64_tr_b16 v[122:123], v54 offset:13824\n"
      "v_mfma_f32_16x16x32_bf16 v[76:79], v[50:53], v[132:135], v[76:79]\n"
      "ds_read_b64_tr_b16 v[124:125], v54 offset:15360\n"
      "ds_read_b64_tr_b16 v[126:127], v54 offset:16896\n"
      "s_waitcnt lgkmcnt(8)\n"
      "v_mfma_f32_16x16x32_bf16 v[76:79], v[50:53], v[104:107], v[76:79]\n"
      "ds_read_b64_tr_b16 v[128:129], v54 offset:18432\n"
      "ds_read_b64_tr_b16 v[130:131], v54 offset:19968\n"
      "v_mfma_f32_16x16x32_bf16 v[76:79], v[50:53], v[108:111], v[76:79]\n"
      "ds_read_b64_tr_b16 v[132:133], v54 offset:21504\n"
      "ds_read_b64_tr_b16 v[134:135], v54 offset:23040\n"
      "s_waitcnt lgkmcnt(8)\n"
      "v_mfma_f32_16x16x32_bf16 v[76:79], v[50:53], v[112:115], v[76:79]\n"
      "ds_read_b64_tr_b16 v[104:105], v54 offset:24576\n"
      "ds_read_b64_tr_b16 v[106:107], v54 offset:26112\n"
      "v_mfma_f32_16x16x32_bf16 v[76:79], v[50:53], v[116:119], v[76:79]\n"
      "ds_read_b64_tr_b16 v[108:109], v54 offset:27648\n"
      "ds_read_b64_tr_b16 v[110:111], v54 offset:29184\n"
      "s_waitcnt vmcnt(0)\n"
      "s_barrier\n"
      "buffer_store_dwordx4 v[96:99], v102, s[24:27], s51 offen sc1\n"
      "s_add_u32 s51, s51, 65536\n"
      "s_waitcnt lgkmcnt(8)\n"
      "v_mfma_f32_16x16x32_bf16 v[76:79], v[50:53], v[120:123], v[76:79]\n"
      "ds_read_b64_tr_b16 v[112:113], v54 offset:30720\n"
      "ds_read_b64_tr_b16 v[114:115], v54 offset:32256\n"
      "v_mfma_f32_16x16x32_bf16 v[76:79], v[50:53], v[124:127], v[76:79]\n"
      "ds_read_b64_tr_b16 v[116:117], v54 offset:33792\n"
      "ds_read_b64_tr_b16 v[118:119], v54 offset:35328\n"
      "s_waitcnt lgkmcnt(8)\n"
      "v_mfma_f32_16x16x32_bf16 v[76:79], v[50:53], v[128:131], v[76:79]\n"
      "ds_read_b64_tr_b16 v[120:121], v54 offset:36864\n"
      "ds_read_b64_tr_b16 v[122:123], v54 offset:38400\n"
      "v_mfma_f32_16x16x32_bf16 v[76:79], v[50:53], v[132:135], v[76:79]\n"
      "ds_read_b64_tr_b16 v[124:125], v54 offset:39936\n"
      "ds_read_b64_tr_b16 v[126:127], v54 offset:41472\n"
      "s_waitcnt lgkmcnt(8)\n"
      "v_mfma_f32_16x16x32_bf16 v[76:79], v[50:53], v[104:107], v[76:79]\n"
      "ds_read_b64_tr_b16 v[128:129], v54 offset:43008\n"
      "ds_read_b64_tr_b16 v[130:131], v54 offset:44544\n"
      "v_mfma_f32_16x16x32_bf16 v[76:79], v[50:53], v[108:111], v[76:79]\n"
      "ds_read_b64_tr_b16 v[132:133], v54 offset:46080\n"
      "ds_read_b64_tr_b16 v[134:135], v54 offset:47616\n"
      "s_waitcnt lgkmcnt(8)\n"
      "v_mfma_f32_16x16x32_bf16 v[76:79], v[50:53], v[112:115], v[76:79]\n"
      "v_mfma_f32_16x16x32_bf16 v[76:79], v[50:53], v[116:119], v[76:79]\n"
      "s_waitcnt lgkmcnt(4)\n"
      "v_mfma_f32_16x16x32_bf16 v[76:79], v[50:53], v[120:123], v[76:79]\n"
      "v_mfma_f32_16x16x32_bf16 v[76:79], v[50:53], v[124:127], v[76:79]\n"
      "s_waitcnt lgkmcnt(0)\n"
      "v_mfma_f32_16x16x32_bf16 v[76:79], v[50:53], v[128:131], v[76:79]\n"
      "v_mfma_f32_16x16x32_bf16 v[76:79], v[50:53], v[132:135], v[76:79]\n"
      "s_nop 7\n"
      "v_cvt_pk_bf16_f32 v77, v76, v76\n"
      "v_lshlrev_b32 v77, 16, v77\n"
      "v_sub_f32 v78, v76, v77\n"
      "v_cvt_pk_bf16_f32 v50, v77, v78\n"
      "v_lshrrev_b32 v51, 16, v77\n"
      "v_mov_b32 v52, 0\n"
      "v_mov_b32 v53, 0\n"
      "ds_write_b128 v55, v[50:53] offset:0\n"
      "ds_read_b64_tr_b16 v[56:57], v43 offset:0\n"
      "ds_read_b64_tr_b16 v[58:59], v43 offset:1536\n"
      "ds_read_b64_tr_b16 v[60:61], v43 offset:64\n"
      "ds_read_b64_tr_b16 v[62:63], v43 offset:1600\n"
      "ds_read_b64_tr_b16 v[64:65], v43 offset:128\n"
      "ds_read_b64_tr_b16 v[66:67], v43 offset:1664\n"
      "ds_read_b64_tr_b16 v[68:69], v43 offset:3072\n"
      "ds_read_b64_tr_b16 v[70:71], v43 offset:4608\n"
      "s_mov_b32 s60, 0x100001\n"
      ".Lspoll_2012_%=:\n"
      "s_load_dwordx2 s[12:13], s[10:11], 0x0 glc\n"
      "s_load_dwordx2 s[14:15], s[10:11], 0x80 glc\n"
      "s_load_dwordx2 s[16:17], s[10:11], 0x100 glc\n"
      "s_load_dwordx2 s[18:19], s[10:11], 0x180 glc\n"
      ".Lscheck_2013_%=:\n"
      "s_sub_u32 s60, s60, 1\n"
      "s_waitcnt lgkmcnt(0)\n"
      "s_cbranch_scc1 .Lspollend_2014_%=\n"
      "s_cmp_eq_u64 s[12:13], s[36:37]\n"
      "s_cbranch_scc0 .Lspoll_2012_%=\n"
      "s_cmp_eq_u64 s[14:15], s[36:37]\n"
      "s_cbranch_scc0 .Lspoll_2012_%=\n"
      "s_cmp_eq_u64 s[16:17], s[36:37]\n"
      "s_cbranch_scc0 .Lspoll_2012_%=\n"
      "s_cmp_eq_u64 s[18:19], s[36:37]\n"
      "s_cbranch_scc0 .Lspoll_2012_%=\n"
      ".Lspollend_2014_%=:\n"
      "s_barrier\n"
      "buffer_load_dwordx4 v[50:53], v2, s[24:27], s64 offen sc1\n"
      "buffer_load_dwordx4 v[154:157], v2, s[24:27], s64 offen offset:1024 sc1\n"
      "buffer_load_dwordx4 v[158:161], v2, s[24:27], s64 offen offset:2048 sc1\n"
      "buffer_load_dwordx4 v[162:165], v2, s[24:27], s64 offen offset:3072 sc1\n"
      "buffer_load_dwordx4 v[166:169], v2, s[24:27], s65 offen sc1\n"
      "buffer_load_dwordx4 v[170:173], v2, s[24:27], s65 offen offset:1024 sc1\n"
      "buffer_load_dwordx4 v[174:177], v2, s[24:27], s65 offen offset:2048 sc1\n"
      "buffer_load_dwordx4 v[178:181], v2, s[24:27], s65 offen offset:3072 sc1\n"
      "s_waitcnt vmcnt(7)\n"
      "v_mfma_f32_32x32x16_bf16 v[76:91], v[56:59], v[50:53], 0\n"
      "ds_read_b64_tr_b16 v[72:73], v43 offset:3136\n"
      "ds_read_b64_tr_b16 v[74:75], v43 offset:4672\n"
      "buffer_load_dwordx4 v[182:185], v2, s[24:27], s66 offen sc1\n"
      "v_mfma_f32_32x32x16_bf16 v[104:119], v[60:63], v[50:53], 0\n"
      "ds_read_b64_tr_b16 v[56:57], v43 offset:3200\n"
      "ds_read_b64_tr_b16 v[58:59], v43 offset:4736\n"
      "v_mfma_f32_32x32x16_bf16 v[120:135], v[64:67], v[50:53], 0\n"
      "ds_read_b64_tr_b16 v[60:61], v43 offset:6144\n"
      "ds_read_b64_tr_b16 v[62:63], v43 offset:7680\n"
      "s_waitcnt vmcnt(7)\n"
      "v_mfma_f32_32x32x16_bf16 v[76:91], v[68:71], v[154:157], v[76:91]\n"
      "ds_read_b64_tr_b16 v[64:65], v43 offset:6208\n"
      "ds_read_b64_tr_b16 v[66:67], v43 offset:7744\n"
      "buffer_load_dwordx4 v[186:189], v2, s[24:27], s66 offen offset:1024 sc1\n"
      "s_waitcnt lgkmcnt(6)\n"
      "v_mfma_f32_32x32x16_bf16 v[104:119], v[72:75], v[154:157], v[104:119]\n"
      "ds_read_b64_tr_b16 v[68:69], v43 offset:6272\n"
      "ds_read_b64_tr_b16 v[70:71], v43 offset:7808\n"
      "s_waitcnt lgkmcnt(6)\n"
      "v_mfma_f32_32x32x16_bf16 v[120:135], v[56:59], v[154:157], v[120:135]\n"
      "ds_read_b64_tr_b16 v[72:73], v43 offset:9216\n"
      "ds_read_b64_tr_b16 v[74:75], v43 offset:10752\n"
      "buffer_load_dwordx4 v[190:193], v2, s[24:27], s66 offen offset:2048 sc1\n"
      "s_waitcnt vmcnt(8) lgkmcnt(6)\n"
      "v_mfma_f32_32x32x16_bf16 v[76:91], v[60:63], v[158:161], v[76:91]\n"
      "ds_read_b64_tr_b16 v[56:57], v43 offset:9280\n"
      "ds_read_b64_tr_b16 v[58:59], v43 offset:10816\n"
      "s_waitcnt lgkmcnt(6)\n"
      "v_mfma_f32_32x32x16_bf16 v[104:119], v[64:67], v[158:161], v[104:119]\n"
      "ds_read_b64_tr_b16 v[60:61], v43 offset:9344\n"
      "ds_read_b64_tr_b16 v[62:63], v43 offset:10880\n"
      "buffer_load_dwordx4 v[194:197], v2, s[24:27], s66 offen offset:3072 sc1\n"
      "s_waitcnt lgkmcnt(6)\n"
      "v_mfma_f32_32x32x16_bf16 v[120:135], v[68:71], v[158:161], v[120:135]\n"
      "ds_read_b64_tr_b16 v[64:65], v43 offset:12288\n"
      "ds_read_b64_tr_b16 v[66:67], v43 offset:13824\n"
      "s_waitcnt vmcnt(8) lgkmcnt(6)\n"
      "v_mfma_f32_32x32x16_bf16 v[76:91], v[72:75], v[162:165], v[76:91]\n"
      "ds_read_b64_tr_b16 v[68:69], v43 offset:12352\n"
      "ds_read_b64_tr_b16 v[70:71], v43 offset:13888\n"
      "buffer_load_dwordx4 v[198:201], v2, s[24:27], s67 offen sc1\n"
      "s_waitcnt lgkmcnt(6)\n"
      "v_mfma_f32_32x32x16_bf16 v[104:119], v[56:59], v[162:165], v[104:119]\n"
      "ds_read_b64_tr_b16 v[72:73], v43 offset:12416\n"
      "ds_read_b64_tr_b16 v[74:75], v43 offset:13952\n"
      "s_waitcnt lgkmcnt(6)\n"
      "v_mfma_f32_32x32x16_bf16 v[120:135], v[60:63], v[162:165], v[120:135]\n"
      "ds_read_b64_tr_b16 v[56:57], v43 offset:15360\n"
      "ds_read_b64_tr_b16 v[58:59], v43 offset:16896\n"
      "s_waitcnt vmcnt(8) lgkmcnt(6)\n"
      "v_mfma_f32_32x32x16_bf16 v[76:91], v[64:67], v[166:169], v[76:91]\n"
      "ds_read_b64_tr_b16 v[60:61], v43 offset:15424\n"
      "ds_read_b64_tr_b16 v[62:63], v43 offset:16960\n"
      "buffer_load_dwordx4 v[202:205], v2, s[24:27], s67 offen offset:1024 sc1\n"
      "s_waitcnt lgkmcnt(6)\n"
      "v_mfma_f32_32x32x16_bf16 v[104:119], v[68:71], v[166:169], v[104:119]\n"
      "ds_read_b64_tr_b16 v[64:65], v43 offset:15488\n"
      "ds_read_b64_tr_b16 v[66:67], v43 offset:17024\n"
      "s_waitcnt lgkmcnt(6)\n"
      "v_mfma_f32_32x32x16_bf16 v[120:135], v[72:75], v[166:169], v[120:135]\n"
      "ds_read_b64_tr_b16 v[68:69], v43 offset:18432\n"
      "ds_read_b64_tr_b16 v[70:71], v43 offset:19968\n"
      "buffer_load_dwordx4 v[206:209], v2, s[24:27], s67 offen offset:2048 sc1\n"
      "s_waitcnt vmcnt(9) lgkmcnt(6)\n"
      "v_mfma_f32_32x32x16_bf16 v[76:91], v[56:59], v[170:173], v[76:91]\n"
      "ds_read_b64_tr_b16 v[72:73], v43 offset:18496\n"
      "ds_read_b64_tr_b16 v[74:75], v43 offset:20032\n"
      "s_waitcnt lgkmcnt(6)\n"
      "v_mfma_f32_32x32x16_bf16 v[104:119], v[60:63], v[170:173], v[104:119]\n"
      "ds_read_b64_tr_b16 v[56:57], v43 offset:18560\n"
      "ds_read_b64_tr_b16 v[58:59], v43 offset:20096\n"
      "buffer_load_dwordx4 v[50:53], v2, s[24:27], s67 offen offset:3072 sc1\n"
      "s_waitcnt lgkmcnt(6)\n"
      "v_mfma_f32_32x32x16_bf16 v[120:135], v[64:67], v[170:173], v[120:135]\n"
      "ds_read_b64_tr_b16 v[60:61], v43 offset:21504\n"
      "ds_read_b64_tr_b16 v[62:63], v43 offset:23040\n"
      "s_waitcnt vmcnt(9) lgkmcnt(6)\n"
      "v_mfma_f32_32x32x16_bf16 v[76:91], v[68:71], v[174:177], v[76:91]\n"
      "ds_read_b64_tr_b16 v[64:65], v43 offset:21568\n"
      "ds_read_b64_tr_b16 v[66:67], v43 offset:23104\n"
      "buffer_load_dwordx4 v[154:157], v2, s[24:27], s68 offen sc1\n"
      "s_waitcnt lgkmcnt(6)\n"
      "v_mfma_f32_32x32x16_bf16 v[104:119], v[72:75], v[174:177], v[104:119]\n"
      "ds_read_b64_tr_b16 v[68:69], v43 offset:21632\n"
      "ds_read_b64_tr_b16 v[70:71], v43 offset:23168\n"
      "s_waitcnt lgkmcnt(6)\n"
      "v_mfma_f32_32x32x16_bf16 v[120:135], v[56:59], v[174:177], v[120:135]\n"
      "ds_read_b64_tr_b16 v[72:73], v43 offset:24576\n"
      "ds_read_b64_tr_b16 v[74:75], v43 offset:26112\n"
      "s_waitcnt vmcnt(9) lgkmcnt(6)\n"
      "v_mfma_f32_32x32x16_bf16 v[76:91], v[60:63], v[178:181], v[76:91]\n"
      "ds_read_b64_tr_b16 v[56:57], v43 offset:24640\n"
      "ds_read_b64_tr_b16 v[58:59], v43 offset:26176\n"
      "buffer_load_dwordx4 v[158:161], v2, s[24:27], s68 offen offset:1024 sc1\n"
      "s_waitcnt lgkmcnt(6)\n"
      "v_mfma_f32_32x32x16_bf16 v[104:119], v[64:67], v[178:181], v[104:119]\n"
      "ds_read_b64_tr_b16 v[60:61], v43 offset:24704\n"
      "ds_read_b64_tr_b16 v[62:63], v43 offset:26240\n"
      "s_waitcnt lgkmcnt(6)\n"
      "v_mfma_f32_32x32x16_bf16 v[120:135], v[68:71], v[178:181], v[120:135]\n"
      "ds_read_b64_tr_b16 v[64:65], v43 offset:27648\n"
      "ds_read_b64_tr_b16 v[66:67], v43 offset:29184\n"
      "buffer_load_dwordx4 v[162:165], v2, s[24:27], s68 offen offset:2048 sc1\n"
      "s_waitcnt vmcnt(10) lgkmcnt(6)\n"
      "v_mfma_f32_32x32x16_bf16 v[76:91], v[72:75], v[182:185], v[76:91]\n"
      "ds_read_b64_tr_b16 v[68:69], v43 offset:27712\n"
      "ds_read_b64_tr_b16 v[70:71], v43 offset:29248\n"
      "s_waitcnt lgkmcnt(6)\n"
      "v_mfma_f32_32x32x16_bf16 v[104:119], v[56:59], v[182:185], v[104:119]\n"
      "ds_read_b64_tr_b16 v[72:73], v43 offset:27776\n"
      "ds_read_b64_tr_b16 v[74:75], v43 offset:29312\n"
      "buffer_load_dwordx4 v[166:169], v2, s[24:27], s68 offen offset:3072 sc1\n"
      "s_waitcnt lgkmcnt(6)\n"
      "v_mfma_f32_32x32x16_bf16 v[120:135], v[60:63], v[182:185], v[120:135]\n"
      "ds_read_b64_tr_b16 v[56:57], v43 offset:30720\n"
      "ds_read_b64_tr_b16 v[58:59], v43 offset:32256\n"
      "s_waitcnt vmcnt(10) lgkmcnt(6)\n"
      "v_mfma_f32_32x32x16_bf16 v[76:91], v[64:67], v[186:189], v[76:91]\n"
      "ds_read_b64_tr_b16 v[60:61], v43 offset:30784\n"
      "ds_read_b64_tr_b16 v[62:63], v43 offset:32320\n"
      "buffer_load_dwordx4 v[170:173], v2, s[24:27], s69 offen sc1\n"
      "s_waitcnt lgkmcnt(6)\n"
      "v_mfma_f32_32x32x16_bf16 v[104:119], v[68:71], v[186:189], v[104:119]\n"
      "ds_read_b64_tr_b16 v[64:65], v43 offset:30848\n"
      "ds_read_b64_tr_b16 v[66:67], v43 offset:32384\n"
      "s_waitcnt lgkmcnt(6)\n"
      "v_mfma_f32_32x32x16_bf16 v[120:135], v[72:75], v[186:189], v[120:135]\n"
      "ds_read_b64_tr_b16 v[68:69], v43 offset:33792\n"
      "ds_read_b64_tr_b16 v[70:71], v43 offset:35328\n"
      "s_waitcnt vmcnt(10) lgkmcnt(6)\n"
      "v_mfma_f32_32x32x16_bf16 v[76:91], v[56:59], v[190:193], v[76:91]\n"
      "ds_read_b64_tr_b16 v[72:73], v43 offset:33856\n"
      "ds_read_b64_tr_b16 v[74:75], v43 offset:35392\n"
      "buffer_load_dwordx4 v[174:177], v2, s[24:27], s69 offen offset:1024 sc1\n"
      "s_waitcnt lgkmcnt(6)\n"
      "v_mfma_f32_32x32x16_bf16 v[104:119], v[60:63], v[190:193], v[104:119]\n"
      "ds_read_b64_tr_b16 v[56:57], v43 offset:33920\n"
      "ds_read_b64_tr_b16 v[58:59], v43 offset:35456\n"
      "s_waitcnt lgkmcnt(6)\n"
      "v_mfma_f32_32x32x16_bf16 v[120:135], v[64:67], v[190:193], v[120:135]\n"
      "ds_read_b64_tr_b16 v[60:61], v43 offset:36864\n"
      "ds_read_b64_tr_b16 v[62:63], v43 offset:38400\n"
      "buffer_load_dwordx4 v[178:181], v2, s[24:27], s69 offen offset:2048 sc1\n"
      "s_waitcnt vmcnt(11) lgkmcnt(6)\n"
      "v_mfma_f32_32x32x16_bf16 v[76:91], v[68:71], v[194:197], v[76:91]\n"
      "ds_read_b64_tr_b16 v[64:65], v43 offset:36928\n"
      "ds_read_b64_tr_b16 v[66:67], v43 offset:38464\n"
      "s_waitcnt lgkmcnt(6)\n"
      "v_mfma_f32_32x32x16_bf16 v[104:119], v[72:75], v[194:197], v[104:119]\n"
      "ds_read_b64_tr_b16 v[68:69], v43 offset:36992\n"
      "ds_read_b64_tr_b16 v[70:71], v43 offset:38528\n"
      "buffer_load_dwordx4 v[182:185], v2, s[24:27], s69 offen offset:3072 sc1\n"
      "s_waitcnt lgkmcnt(6)\n"
      "v_mfma_f32_32x32x16_bf16 v[120:135], v[56:59], v[194:197], v[120:135]\n"
      "ds_read_b64_tr_b16 v[72:73], v43 offset:39936\n"
      "ds_read_b64_tr_b16 v[74:75], v43 offset:41472\n"
      "s_waitcnt vmcnt(11) lgkmcnt(6)\n"
      "v_mfma_f32_32x32x16_bf16 v[76:91], v[60:63], v[198:201], v[76:91]\n"
      "ds_read_b64_tr_b16 v[56:57], v43 offset:40000\n"
      "ds_read_b64_tr_b16 v[58:59], v43 offset:41536\n"
      "buffer_load_dwordx4 v[186:189], v2, s[24:27], s70 offen sc1\n"
      "s_waitcnt lgkmcnt(6)\n"
      "v_mfma_f32_32x32x16_bf16 v[104:119], v[64:67], v[198:201], v[104:119]\n"
      "ds_read_b64_tr_b16 v[60:61], v43 offset:40064\n"
      "ds_read_b64_tr_b16 v[62:63], v43 offset:41600\n"
      "s_waitcnt lgkmcnt(6)\n"
      "v_mfma_f32_32x32x16_bf16 v[120:135], v[68:71], v[198:201], v[120:135]\n"
      "ds_read_b64_tr_b16 v[64:65], v43 offset:43008\n"
      "ds_read_b64_tr_b16 v[66:67], v43 offset:44544\n"
      "s_waitcnt vmcnt(11) lgkmcnt(6)\n"
      "v_mfma_f32_32x32x16_bf16 v[76:91], v[72:75], v[202:205], v[76:91]\n"
      "ds_read_b64_tr_b16 v[68:69], v43 offset:43072\n"
      "ds_read_b64_tr_b16 v[70:71], v43 offset:44608\n"
      "buffer_load_dwordx4 v[190:193], v2, s[24:27], s70 offen offset:1024 sc1\n"
      "s_waitcnt lgkmcnt(6)\n"
      "v_mfma_f32_32x32x16_bf16 v[104:119], v[56:59], v[202:205], v[104:119]\n"
      "ds_read_b64_tr_b16 v[72:73], v43 offset:43136\n"
      "ds_read_b64_tr_b16 v[74:75], v43 offset:44672\n"
      "s_waitcnt lgkmcnt(6)\n"
      "v_mfma_f32_32x32x16_bf16 v[120:135], v[60:63], v[202:205], v[120:135]\n"
      "ds_read_b64_tr_b16 v[56:57], v43 offset:46080\n"
      "ds_read_b64_tr_b16 v[58:59], v43 offset:47616\n"
      "buffer_load_dwordx4 v[194:197], v2, s[24:27], s70 offen offset:2048 sc1\n"
      "s_waitcnt vmcnt(12) lgkmcnt(6)\n"
      "v_mfma_f32_32x32x16_bf16 v[76:91], v[64:67], v[206:209], v[76:91]\n"
      "ds_read_b64_tr_b16 v[60:61], v43 offset:46144\n"
      "ds_read_b64_tr_b16 v[62:63], v43 offset:47680\n"
      "s_waitcnt lgkmcnt(6)\n"
      "v_mfma_f32_32x32x16_bf16 v[104:119], v[68:71], v[206:209], v[104:119]\n"
      "ds_read_b64_tr_b16 v[64:65], v43 offset:46208\n"
      "ds_read_b64_tr_b16 v[66:67], v43 offset:47744\n"
      "buffer_load_dwordx4 v[198:201], v2, s[24:27], s70 offen offset:3072 sc1\n"
      "s_waitcnt lgkmcnt(6)\n"
      "v_mfma_f32_32x32x16_bf16 v[120:135], v[72:75], v[206:209], v[120:135]\n"
      "ds_read_b64_tr_b16 v[68:69], v44 offset:0\n"
      "ds_read_b64_tr_b16 v[70:71], v44 offset:1536\n"
      "s_waitcnt vmcnt(12) lgkmcnt(6)\n"
      "v_mfma_f32_32x32x16_bf16 v[76:91], v[56:59], v[50:53], v[76:91]\n"
      "ds_read_b64_tr_b16 v[72:73], v44 offset:64\n"
      "ds_read_b64_tr_b16 v[74:75], v44 offset:1600\n"
      "buffer_load_dwordx4 v[202:205], v2, s[24:27], s71 offen sc1\n"
      "s_waitcnt lgkmcnt(6)\n"
      "v_mfma_f32_32x32x16_bf16 v[104:119], v[60:63], v[50:53], v[104:119]\n"
      "ds_read_b64_tr_b16 v[56:57], v44 offset:128\n"
      "ds_read_b64_tr_b16 v[58:59], v44 offset:1664\n"
      "s_waitcnt lgkmcnt(6)\n"
      "v_mfma_f32_32x32x16_bf16 v[120:135], v[64:67], v[50:53], v[120:135]\n"
      "ds_read_b64_tr_b16 v[60:61], v44 offset:3072\n"
      "ds_read_b64_tr_b16 v[62:63], v44 offset:4608\n"
      "s_waitcnt vmcnt(12) lgkmcnt(6)\n"
      "v_mfma_f32_32x32x16_bf16 v[76:91], v[68:71], v[154:157], v[76:91]\n"
      "ds_read_b64_tr_b16 v[64:65], v44 offset:3136\n"
      "ds_read_b64_tr_b16 v[66:67], v44 offset:4672\n"
      "buffer_load_dwordx4 v[206:209], v2, s[24:27], s71 offen offset:1024 sc1\n"
      "s_waitcnt lgkmcnt(6)\n"
      "v_mfma_f32_32x32x16_bf16 v[104:119], v[72:75], v[154:157], v[104:119]\n"
      "ds_read_b64_tr_b16 v[68:69], v44 offset:3200\n"
      "ds_read_b64_tr_b16 v[70:71], v44 offset:4736\n"
      "s_waitcnt lgkmcnt(6)\n"
      "v_mfma_f32_32x32x16_bf16 v[120:135], v[56:59], v[154:157], v[120:135]\n"
      "ds_read_b64_tr_b16 v[72:73], v44 offset:6144\n"
      "ds_read_b64_tr_b16 v[74:75], v44 offset:7680\n"
      "buffer_load_dwordx4 v[50:53], v2, s[24:27], s71 offen offset:2048 sc1\n"
      "s_waitcnt vmcnt(13) lgkmcnt(6)\n"
      "v_mfma_f32_32x32x16_bf16 v[76:91], v[60:63], v[158:161], v[76:91]\n"
      "ds_read_b64_tr_b16 v[56:57], v44 offset:6208\n"
      "ds_read_b64_tr_b16 v[58:59], v44 offset:7744\n"
      "s_waitcnt lgkmcnt(6)\n"
      "v_mfma_f32_32x32x16_bf16 v[104:119], v[64:67], v[158:161], v[104:119]\n"
      "ds_read_b64_tr_b16 v[60:61], v44 offset:6272\n"
      "ds_read_b64_tr_b16 v[62:63], v44 offset:7808\n"
      "buffer_load_dwordx4 v[154:157], v2, s[24:27], s71 offen offset:3072 sc1\n"
      "s_waitcnt lgkmcnt(6)\n"
      "v_mfma_f32_32x32x16_bf16 v[120:135], v[68:71], v[158:161], v[120:135]\n"
      "ds_read_b64_tr_b16 v[64:65], v44 offset:9216\n"
      "ds_read_b64_tr_b16 v[66:67], v44 offset:10752\n"
      "s_barrier\n"
      "s_waitcnt vmcnt(13) lgkmcnt(6)\n"
      "v_mfma_f32_32x32x16_bf16 v[76:91], v[72:75], v[162:165], v[76:91]\n"
      "ds_read_b64_tr_b16 v[68:69], v44 offset:12288\n"
      "ds_read_b64_tr_b16 v[70:71], v44 offset:13824\n"
      "ds_read_b128 v[136:139], v46 offset:0\n"
      "s_waitcnt lgkmcnt(7)\n"
      "v_mfma_f32_32x32x16_bf16 v[104:119], v[56:59], v[162:165], v[104:119]\n"
      "ds_read_b64_tr_b16 v[72:73], v44 offset:15360\n"
      "ds_read_b64_tr_b16 v[74:75], v44 offset:16896\n"
      "ds_read_b128 v[148:151], v47 offset:4096\n"
      "s_waitcnt lgkmcnt(8)\n"
      "v_mfma_f32_32x32x16_bf16 v[120:135], v[60:63], v[162:165], v[120:135]\n"
      "ds_read_b64_tr_b16 v[56:57], v44 offset:18432\n"
      "ds_read_b64_tr_b16 v[58:59], v44 offset:19968\n"
      "ds_read_b128 v[140:143], v46 offset:1024\n"
      "s_waitcnt vmcnt(12) lgkmcnt(9)\n"
      "v_mfma_f32_32x32x16_bf16 v[76:91], v[64:67], v[166:169], v[76:91]\n"
      "ds_read_b64_tr_b16 v[60:61], v44 offset:21504\n"
      "ds_read_b64_tr_b16 v[62:63], v44 offset:23040\n"
      "ds_read_b128 v[144:147], v46 offset:2048\n"
      "s_waitcnt vmcnt(11) lgkmcnt(10)\n"
      "v_mfma_f32_32x32x16_bf16 v[76:91], v[68:71], v[170:173], v[76:91]\n"
      "ds_read_b64_tr_b16 v[64:65], v44 offset:24576\n"
      "ds_read_b64_tr_b16 v[66:67], v44 offset:26112\n"
      "ds_read_b32 v152, v48\n"
      "s_waitcnt vmcnt(10) lgkmcnt(10)\n"
      "v_mfma_f32_32x32x16_bf16 v[76:91], v[72:75], v[174:177], v[76:91]\n"
      "ds_read_b64_tr_b16 v[68:69], v44 offset:27648\n"
      "ds_read_b64_tr_b16 v[70:71], v44 offset:29184\n"
      "s_waitcnt vmcnt(9) lgkmcnt(9)\n"
      "v_mfma_f32_32x32x16_bf16 v[76:91], v[56:59], v[178:181], v[76:91]\n"
      "ds_read_b64_tr_b16 v[72:73], v44 offset:30720\n"
      "ds_read_b64_tr_b16 v[74:75], v44 offset:32256\n"
      "s_waitcnt vmcnt(8) lgkmcnt(8)\n"
      "v_mfma_f32_32x32x16_bf16 v[76:91], v[60:63], v[182:185], v[76:91]\n"
      "ds_read_b64_tr_b16 v[56:57], v44 offset:33792\n"
      "ds_read_b64_tr_b16 v[58:59], v44 offset:35328\n"
      "s_waitcnt vmcnt(7) lgkmcnt(7)\n"
      "v_mfma_f32_32x32x16_bf16 v[76:91], v[64:67], v[186:189], v[76:91]\n"
      "ds_read_b64_tr_b16 v[60:61], v44 offset:36864\n"
      "ds_read_b64_tr_b16 v[62:63], v44 offset:38400\n"
      "s_waitcnt vmcnt(6) lgkmcnt(6)\n"
      "v_mfma_f32_32x32x16_bf16 v[76:91], v[68:71], v[190:193], v[76:91]\n"
      "ds_read_b64_tr_b16 v[64:65], v44 offset:39936\n"
      "ds_read_b64_tr_b16 v[66:67], v44 offset:41472\n"
      "s_waitcnt vmcnt(5) lgkmcnt(6)\n"
      "v_mfma_f32_32x32x16_bf16 v[76:91], v[72:75], v[194:197], v[76:91]\n"
      "ds_read_b64_tr_b16 v[68:69], v44 offset:43008\n"
      "ds_read_b64_tr_b16 v[70:71], v44 offset:44544\n"
      "s_waitcnt vmcnt(4) lgkmcnt(6)\n"
      "v_mfma_f32_32x32x16_bf16 v[76:91], v[56:59], v[198:201], v[76:91]\n"
      "ds_read_b64_tr_b16 v[72:73], v44 offset:46080\n"
      "ds_read_b64_tr_b16 v[74:75], v44 offset:47616\n"
      "s_waitcnt vmcnt(3) lgkmcnt(6)\n"
      "v_mfma_f32_32x32x16_bf16 v[76:91], v[60:63], v[202:205], v[76:91]\n"
      "ds_read_b64_tr_b16 v[56:57], v44 offset:9280\n"
      "ds_read_b64_tr_b16 v[58:59], v44 offset:10816\n"
      "s_waitcnt vmcnt(2) lgkmcnt(6)\n"
      "v_mfma_f32_32x32x16_bf16 v[76:91], v[64:67], v[206:209], v[76:91]\n"
      "ds_read_b64_tr_b16 v[60:61], v44 offset:12352\n"
      "ds_read_b64_tr_b16 v[62:63], v44 offset:13888\n"
      "s_waitcnt vmcnt(1) lgkmcnt(6)\n"
      "v_mfma_f32_32x32x16_bf16 v[76:91], v[68:71], v[50:53], v[76:91]\n"
      "ds_read_b64_tr_b16 v[64:65], v44 offset:15424\n"
      "ds_read_b64_tr_b16 v[66:67], v44 offset:16960\n"
      "s_waitcnt vmcnt(0) lgkmcnt(6)\n"
      "v_mfma_f32_32x32x16_bf16 v[76:91], v[72:75], v[154:157], v[76:91]\n"
      "ds_read_b64_tr_b16 v[68:69], v44 offset:18496\n"
      "ds_read_b64_tr_b16 v[70:71], v44 offset:20032\n"
      "v_mfma_f32_32x32x16_bf16 v[76:91], v[136:139], v[148:151], v[76:91]\n"
      "s_waitcnt lgkmcnt(6)\n"
      "v_mfma_f32_32x32x16_bf16 v[104:119], v[56:59], v[166:169], v[104:119]\n"
      "ds_read_b64_tr_b16 v[72:73], v44 offset:21568\n"
      "ds_read_b64_tr_b16 v[74:75], v44 offset:23104\n"
      "s_waitcnt lgkmcnt(6)\n"
      "v_mfma_f32_32x32x16_bf16 v[104:119], v[60:63], v[170:173], v[104:119]\n"
      "ds_read_b64_tr_b16 v[56:57], v44 offset:24640\n"
      "ds_read_b64_tr_b16 v[58:59], v44 offset:26176\n"
      "s_waitcnt lgkmcnt(6)\n"
      "v_mfma_f32_32x32x16_bf16 v[104:119], v[64:67], v[174:177], v[104:119]\n"
      "ds_read_b64_tr_b16 v[60:61], v44 offset:27712\n"
      "ds_read_b64_tr_b16 v[62:63], v44 offset:29248\n"
      "v_max_f32 v76, 0, v76\n"
      "v_max_f32 v77, 0, v77\n"
      "v_mul_f32 v76, v76, v76\n"
      "v_mul_f32 v77, v77, v77\n"
      "s_waitcnt lgkmcnt(6)\n"
      "v_mfma_f32_32x32x16_bf16 v[104:119], v[68:71], v[178:181], v[104:119]\n"
      "ds_read_b64_tr_b16 v[64:65], v44 offset:30784\n"
      "ds_read_b64_tr_b16 v[66:67], v44 offset:32320\n"
      "v_cvt_pk_bf16_f32 v76, v76, v77\n"
      "v_max_f32 v78, 0, v78\n"
      "v_max_f32 v79, 0, v79\n"
      "v_mul_f32 v78, v78, v78\n"
      "s_waitcnt lgkmcnt(6)\n"
      "v_mfma_f32_32x32x16_bf16 v[104:119], v[72:75], v[182:185], v[104:119]\n"
      "ds_read_b64_tr_b16 v[68:69], v44 offset:33856\n"
      "ds_read_b64_tr_b16 v[70:71], v44 offset:35392\n"
      "v_mul_f32 v79, v79, v79\n"
      "v_cvt_pk_bf16_f32 v77, v78, v79\n"
      "v_max_f32 v80, 0, v80\n"
      "v_max_f32 v81, 0, v81\n"
      "s_waitcnt lgkmcnt(6)\n"
      "v_mfma_f32_32x32x16_bf16 v[104:119], v[56:59], v[186:189], v[104:119]\n"
      "ds_read_b64_tr_b16 v[72:73], v44 offset:36928\n"
      "ds_read_b64_tr_b16 v[74:75], v44 offset:38464\n"
      "v_mul_f32 v80, v80, v80\n"
      "v_mul_f32 v81, v81, v81\n"
      "v_cvt_pk_bf16_f32 v78, v80, v81\n"
      "v_max_f32 v82, 0, v82\n"
      "s_waitcnt lgkmcnt(6)\n"
      "v_mfma_f32_32x32x16_bf16 v[104:119], v[60:63], v[190:193], v[104:119]\n"
      "ds_read_b64_tr_b16 v[56:57], v44 offset:40000\n"
      "ds_read_b64_tr_b16 v[58:59], v44 offset:41536\n"
      "v_max_f32 v83, 0, v83\n"
      "v_mul_f32 v82, v82, v82\n"
      "v_mul_f32 v83, v83, v83\n"
      "v_cvt_pk_bf16_f32 v79, v82, v83\n"
      "s_waitcnt lgkmcnt(6)\n"
      "v_mfma_f32_32x32x16_bf16 v[104:119], v[64:67], v[194:197], v[104:119]\n"
      "ds_read_b64_tr_b16 v[60:61], v44 offset:43072\n"
      "ds_read_b64_tr_b16 v[62:63], v44 offset:44608\n"
      "buffer_store_dwordx4 v[76:79], v2, s[24:27], s4 offen sc1\n"
      "v_max_f32 v84, 0, v84\n"
      "v_max_f32 v85, 0, v85\n"
      "v_mul_f32 v84, v84, v84\n"
      "s_waitcnt lgkmcnt(6)\n"
      "v_mfma_f32_32x32x16_bf16 v[104:119], v[68:71], v[198:201], v[104:119]\n"
      "ds_read_b64_tr_b16 v[64:65], v44 offset:46144\n"
      "ds_read_b64_tr_b16 v[66:67], v44 offset:47680\n"
      "v_mul_f32 v85, v85, v85\n"
      "v_cvt_pk_bf16_f32 v84, v84, v85\n"
      "v_max_f32 v86, 0, v86\n"
      "v_max_f32 v87, 0, v87\n"
      "s_waitcnt lgkmcnt(6)\n"
      "v_mfma_f32_32x32x16_bf16 v[104:119], v[72:75], v[202:205], v[104:119]\n"
      "ds_read_b64_tr_b16 v[68:69], v44 offset:9344\n"
      "ds_read_b64_tr_b16 v[70:71], v44 offset:10880\n"
      "v_mul_f32 v86, v86, v86\n"
      "v_mul_f32 v87, v87, v87\n"
      "v_cvt_pk_bf16_f32 v85, v86, v87\n"
      "v_max_f32 v88, 0, v88\n"
      "s_waitcnt lgkmcnt(6)\n"
      "v_mfma_f32_32x32x16_bf16 v[104:119], v[56:59], v[206:209], v[104:119]\n"
      "ds_read_b64_tr_b16 v[72:73], v44 offset:12416\n"
      "ds_read_b64_tr_b16 v[74:75], v44 offset:13952\n"
      "v_max_f32 v89, 0, v89\n"
      "v_mul_f32 v88, v88, v88\n"
      "v_mul_f32 v89, v89, v89\n"
      "v_cvt_pk_bf16_f32 v86, v88, v89\n"
      "s_waitcnt lgkmcnt(6)\n"
      "v_mfma_f32_32x32x16_bf16 v[104:119], v[60:63], v[50:53], v[104:119]\n"
      "ds_read_b64_tr_b16 v[56:57], v44 offset:15488\n"
      "ds_read_b64_tr_b16 v[58:59], v44 offset:17024\n"
      "v_max_f32 v90, 0, v90\n"
      "v_max_f32 v91, 0, v91\n"
      "v_mul_f32 v90, v90, v90\n"
      "v_mul_f32 v91, v91, v91\n"
      "s_waitcnt lgkmcnt(6)\n"
      "v_mfma_f32_32x32x16_bf16 v[104:119], v[64:67], v[154:157], v[104:119]\n"
      "ds_read_b64_tr_b16 v[60:61], v44 offset:18560\n"
      "ds_read_b64_tr_b16 v[62:63], v44 offset:20096\n"
      "v_cvt_pk_bf16_f32 v87, v90, v91\n"
      "buffer_store_dwordx4 v[84:87], v2, s[24:27], s5 offen sc1\n"
      "v_mfma_f32_32x32x16_bf16 v[104:119], v[140:143], v[148:151], v[104:119]\n"
      "s_waitcnt lgkmcnt(6)\n"
      "v_mfma_f32_32x32x16_bf16 v[120:135], v[68:71], v[166:169], v[120:135]\n"
      "ds_read_b64_tr_b16 v[64:65], v44 offset:21632\n"
      "ds_read_b64_tr_b16 v[66:67], v44 offset:23168\n"
      "s_waitcnt lgkmcnt(6)\n"
      "v_mfma_f32_32x32x16_bf16 v[120:135], v[72:75], v[170:173], v[120:135]\n"
      "ds_read_b64_tr_b16 v[68:69], v44 offset:24704\n"
      "ds_read_b64_tr_b16 v[70:71], v44 offset:26240\n"
      "s_waitcnt lgkmcnt(6)\n"
      "s_nop 0\n"
      ".Lpage7_%=:\n"
      "s_cbranch_execz .Lret_%=\n"
      "v_mfma_f32_32x32x16_bf16 v[120:135], v[56:59], v[174:177], v[120:135]\n"
      "ds_read_b64_tr_b16 v[72:73], v44 offset:27776\n"
      "ds_read_b64_tr_b16 v[74:75], v44 offset:29312\n"
      "v_max_f32 v104, 0, v104\n"
      "v_max_f32 v105, 0, v105\n"
      "v_mul_f32 v104, v104, v104\n"
      "v_mul_f32 v105, v105, v105\n"
      "s_waitcnt lgkmcnt(6)\n"
      "v_mfma_f32_32x32x16_bf16 v[120:135], v[60:63], v[178:181], v[120:135]\n"
      "ds_read_b64_tr_b16 v[56:57], v44 offset:30848\n"
      "ds_read_b64_tr_b16 v[58:59], v44 offset:32384\n"
      "v_cvt_pk_bf16_f32 v104, v104, v105\n"
      "v_max_f32 v106, 0, v106\n"
      "v_max_f32 v107, 0, v107\n"
      "v_mul_f32 v106, v106, v106\n"
      "s_waitcnt lgkmcnt(6)\n"
      "v_mfma_f32_32x32x16_bf16 v[120:135], v[64:67], v[182:185], v[120:135]\n"
      "ds_read_b64_tr_b16 v[60:61], v44 offset:33920\n"
      "ds_read_b64_tr_b16 v[62:63], v44 offset:35456\n"
      "v_mul_f32 v107, v107, v107\n"
      "v_cvt_pk_bf16_f32 v105, v106, v107\n"
      "v_max_f32 v108, 0, v108\n"
      "v_max_f32 v109, 0, v109\n"
      "s_waitcnt lgkmcnt(6)\n"
      "v_mfma_f32_32x32x16_bf16 v[120:135], v[68:71], v[186:189], v[120:135]\n"
      "ds_read_b64_tr_b16 v[64:65], v44 offset:36992\n"
      "ds_read_b64_tr_b16 v[66:67], v44 offset:38528\n"
      "v_mul_f32 v108, v108, v108\n"
      "v_mul_f32 v109, v109, v109\n"
      "v_cvt_pk_bf16_f32 v106, v108, v109\n"
      "v_max_f32 v110, 0, v110\n"
      "s_waitcnt lgkmcnt(6)\n"
      "v_mfma_f32_32x32x16_bf16 v[120:135], v[72:75], v[190:193], v[120:135]\n"
      "ds_read_b64_tr_b16 v[68:69], v44 offset:40064\n"
      "ds_read_b64_tr_b16 v[70:71], v44 offset:41600\n"
      "v_max_f32 v111, 0, v111\n"
      "v_mul_f32 v110, v110, v110\n"
      "v_mul_f32 v111, v111, v111\n"
      "v_cvt_pk_bf16_f32 v107, v110, v111\n"
      "s_waitcnt lgkmcnt(6)\n"
      "v_mfma_f32_32x32x16_bf16 v[120:135], v[56:59], v[194:197], v[120:135]\n"
      "ds_read_b64_tr_b16 v[72:73], v44 offset:43136\n"
      "ds_read_b64_tr_b16 v[74:75], v44 offset:44672\n"
      "buffer_store_dwordx4 v[104:107], v2, s[24:27], s6 offen sc1\n"
      "v_max_f32 v112, 0, v112\n"
      "v_max_f32 v113, 0, v113\n"
      "v_mul_f32 v112, v112, v112\n"
      "s_waitcnt lgkmcnt(6)\n"
      "v_mfma_f32_32x32x16_bf16 v[120:135], v[60:63], v[198:201], v[120:135]\n"
      "ds_read_b64_tr_b16 v[56:57], v44 offset:46208\n"
      "ds_read_b64_tr_b16 v[58:59], v44 offset:47744\n"
      "v_mul_f32 v113, v113, v113\n"
      "v_cvt_pk_bf16_f32 v112, v112, v113\n"
      "v_max_f32 v114, 0, v114\n"
      "v_max_f32 v115, 0, v115\n"
      "s_waitcnt lgkmcnt(6)\n"
      "v_mfma_f32_32x32x16_bf16 v[120:135], v[64:67], v[202:205], v[120:135]\n"
      "v_mul_f32 v114, v114, v114\n"
      "v_mul_f32 v115, v115, v115\n"
      "v_cvt_pk_bf16_f32 v113, v114, v115\n"
      "v_max_f32 v116, 0, v116\n"
      "s_waitcnt lgkmcnt(4)\n"
      "v_mfma_f32_32x32x16_bf16 v[120:135], v[68:71], v[206:209], v[120:135]\n"
      "v_max_f32 v117, 0, v117\n"
      "v_mul_f32 v116, v116, v116\n"
      "v_mul_f32 v117, v117, v117\n"
      "v_cvt_pk_bf16_f32 v114, v116, v117\n"
      "s_waitcnt lgkmcnt(2)\n"
      "v_mfma_f32_32x32x16_bf16 v[120:135], v[72:75], v[50:53], v[120:135]\n"
      "v_max_f32 v118, 0, v118\n"
      "v_max_f32 v119, 0, v119\n"
      "v_mul_f32 v118, v118, v118\n"
      "v_mul_f32 v119, v119, v119\n"
      "s_waitcnt lgkmcnt(0)\n"
      "v_mfma_f32_32x32x16_bf16 v[120:135], v[56:59], v[154:157], v[120:135]\n"
      "v_cvt_pk_bf16_f32 v115, v118, v119\n"
      "buffer_store_dwordx4 v[112:115], v2, s[24:27], s7 offen sc1\n"
      "v_mfma_f32_32x32x16_bf16 v[120:135], v[144:147], v[148:151], v[120:135]\n"
      "s_nop 11\n"
      "v_mul_f32 v120, v120, |v120|\n"
      "v_mul_f32 v121, v121, |v121|\n"
      "v_cvt_pk_bf16_f32 v120, v120, v121\n"
      "v_pk_max_i16 v120, v120, 0\n"
      "v_mul_f32 v122, v122, |v122|\n"
      "v_mul_f32 v123, v123, |v123|\n"
      "v_cvt_pk_bf16_f32 v121, v122, v123\n"
      "v_pk_max_i16 v121, v121, 0\n"
      "v_mul_f32 v124, v124, |v124|\n"
      "v_mul_f32 v125, v125, |v125|\n"
      "v_cvt_pk_bf16_f32 v122, v124, v125\n"
      "v_pk_max_i16 v122, v122, 0\n"
      "v_mul_f32 v126, v126, |v126|\n"
      "v_mul_f32 v127, v127, |v127|\n"
      "v_cvt_pk_bf16_f32 v123, v126, v127\n"
      "v_pk_max_i16 v123, v123, 0\n"
      "buffer_store_dwordx4 v[120:123], v45, s[24:27], s8 offen sc1\n"
      "v_mul_f32 v128, v128, |v128|\n"
      "v_mul_f32 v129, v129, |v129|\n"
      "v_cvt_pk_bf16_f32 v128, v128, v129\n"
      "v_pk_max_i16 v128, v128, 0\n"
      "v_mul_f32 v130, v130, |v130|\n"
      "v_mul_f32 v131, v131, |v131|\n"
      "v_cvt_pk_bf16_f32 v129, v130, v131\n"
      "v_pk_max_i16 v129, v129, 0\n"
      "v_mul_f32 v132, v132, |v132|\n"
      "v_mul_f32 v133, v133, |v133|\n"
      "v_cvt_pk_bf16_f32 v130, v132, v133\n"
      "v_pk_max_i16 v130, v130, 0\n"
      "v_mul_f32 v134, v134, |v134|\n"
      "v_mul_f32 v135, v135, |v135|\n"
      "v_cvt_pk_bf16_f32 v131, v134, v135\n"
      "v_pk_max_i16 v131, v131, 0\n"
      "buffer_store_dwordx4 v[128:131], v45, s[24:27], s9 offen sc1\n"
      "s_barrier\n"
      "v_cmp_eq_u32 vcc, 1, v6\n"
      "v_mov_b32 v34, s82\n"
      "v_mov_b32 v35, s80\n"
      "v_cndmask_b32 v34, v35, v34, vcc\n"
      "v_lshlrev_b32 v32, 16, v34\n"
      "v_and_b32 v33, 0xffff0000, v34\n"
      "v_pk_add_f32 v[8:9], v[8:9], v[32:33]\n"
      "v_mov_b32 v34, s83\n"
      "v_mov_b32 v35, s81\n"
      "v_cndmask_b32 v34, v35, v34, vcc\n"
      "v_lshlrev_b32 v32, 16, v34\n"
      "v_and_b32 v33, 0xffff0000, v34\n"
      "v_pk_add_f32 v[10:11], v[10:11], v[32:33]\n"
      "v_mov_b32 v34, s86\n"
      "v_mov_b32 v35, s84\n"
      "v_cndmask_b32 v34, v35, v34, vcc\n"
      "v_lshlrev_b32 v32, 16, v34\n"
      "v_and_b32 v33, 0xffff0000, v34\n"
      "v_pk_add_f32 v[12:13], v[12:13], v[32:33]\n"
      "v_mov_b32 v34, s87\n"
      "v_mov_b32 v35, s85\n"
      "v_cndmask_b32 v34, v35, v34, vcc\n"
      "v_lshlrev_b32 v32, 16, v34\n"
      "v_and_b32 v33, 0xffff0000, v34\n"
      "v_pk_add_f32 v[14:15], v[14:15], v[32:33]\n"
      "v_mov_b32 v34, s90\n"
      "v_mov_b32 v35, s88\n"
      "v_cndmask_b32 v34, v35, v34, vcc\n"
      "v_lshlrev_b32 v32, 16, v34\n"
      "v_and_b32 v33, 0xffff0000, v34\n"
      "v_pk_add_f32 v[16:17], v[16:17], v[32:33]\n"
      "v_mov_b32 v34, s91\n"
      "v_mov_b32 v35, s89\n"
      "v_cndmask_b32 v34, v35, v34, vcc\n"
      "v_lshlrev_b32 v32, 16, v34\n"
      "v_and_b32 v33, 0xffff0000, v34\n"
      "v_pk_add_f32 v[18:19], v[18:19], v[32:33]\n"
      "v_mov_b32 v34, s94\n"
      "v_mov_b32 v35, s92\n"
      "v_cndmask_b32 v34, v35, v34, vcc\n"
      "v_lshlrev_b32 v32, 16, v34\n"
      "v_and_b32 v33, 0xffff0000, v34\n"
      "v_pk_add_f32 v[20:21], v[20:21], v[32:33]\n"
      "v_mov_b32 v34, s95\n"
      "v_mov_b32 v35, s93\n"
      "v_cndmask_b32 v34, v35, v34, vcc\n"
      "v_lshlrev_b32 v32, 16, v34\n"
      "v_and_b32 v33, 0xffff0000, v34\n"
      "v_pk_add_f32 v[22:23], v[22:23], v[32:33]\n"
      "v_pk_mul_f32 v[8:9], v[8:9], v[152:153] op_sel_hi:[1,0]\n"
      "v_pk_mul_f32 v[10:11], v[10:11], v[152:153] op_sel_hi:[1,0]\n"
      "v_pk_mul_f32 v[12:13], v[12:13], v[152:153] op_sel_hi:[1,0]\n"
      "v_pk_mul_f32 v[14:15], v[14:15], v[152:153] op_sel_hi:[1,0]\n"
      "v_pk_mul_f32 v[16:17], v[16:17], v[152:153] op_sel_hi:[1,0]\n"
      "v_pk_mul_f32 v[18:19], v[18:19], v[152:153] op_sel_hi:[1,0]\n"
      "v_pk_mul_f32 v[20:21], v[20:21], v[152:153] op_sel_hi:[1,0]\n"
      "v_pk_mul_f32 v[22:23], v[22:23], v[152:153] op_sel_hi:[1,0]\n"
      "v_rcp_f32 v152, v152\n"
      "s_mul_i32 s52, s50, 24576\n"
      "v_add_u32 v7, s52, v92\n"
      "v_xor_b32 v40, 64, v7\n"
      "s_add_u32 s52, s52, 49152\n"
      "v_add_u32 v43, s52, v7\n"
      "v_add_u32 v44, s52, v40\n"
      "s_mul_i32 s64, s75, 196608\n"
      "s_mul_i32 s59, s50, 98304\n"
      "s_add_u32 s64, s64, s59\n"
      "s_add_u32 s64, s64, s76\n"
      "s_add_u32 s64, s64, 1114112\n"
      "s_add_u32 s65, s64, 4096\n"
      "s_add_u32 s66, s64, 16384\n"
      "s_add_u32 s67, s64, 20480\n"
      "s_add_u32 s68, s64, 32768\n"
      "s_add_u32 s69, s64, 36864\n"
      "s_add_u32 s70, s64, 49152\n"
      "s_add_u32 s71, s64, 53248\n"
      "s_add_u32 s72, s64, 65536\n"
      "s_add_u32 s73, s64, 69632\n"
      "s_add_u32 s74, s64, 81920\n"
      "v_lshlrev_b32 v45, 10, v5\n"
      "v_lshl_add_u32 v45, v6, 4, v45\n"
      "s_lshl_b32 s62, s61, 15\n"
      "s_lshl_b32 s53, s40, 7\n"
      "s_add_u32 s62, s62, s53\n"
      "s_lshl_b32 s53, s50, 6\n"
      "s_add_u32 s62, s62, s53\n"
      "s_lshl_b32 s52, s44, 11\n"
      "s_lshl_b32 s59, s38, 9\n"
      "s_add_u32 s52, s52, s59\n"
      "s_add_u32 s52, s52, 999424\n"
      "s_waitcnt vmcnt(0)\n"
      "s_barrier\n"
      "buffer_store_dwordx4 v[96:99], v102, s[24:27], s51 offen sc1\n"
      "s_add_u32 s51, s51, -65536\n"
      "s_and_b32 s59, s52, 0xffffe000\n"
      "s_add_u32 s52, s52, s59\n"
      "s_add_u32 s52, s52, s76\n"
      "s_add_u32 s10, s24, s52\n"
      "s_addc_u32 s11, s25, 0\n"
      "ds_read_b64_tr_b16 v[56:57], v40 offset:0\n"
      "ds_read_b64_tr_b16 v[58:59], v40 offset:1024\n"
      "ds_read_b64_tr_b16 v[60:61], v7 offset:0\n"
      "ds_read_b64_tr_b16 v[62:63], v7 offset:1024\n"
      "ds_read_b64_tr_b16 v[64:65], v40 offset:2048\n"
      "ds_read_b64_tr_b16 v[66:67], v40 offset:3072\n"
      "ds_read_b64_tr_b16 v[68:69], v7 offset:2048\n"
      "ds_read_b64_tr_b16 v[70:71], v7 offset:3072\n"
      "s_sleep 3\n"
      "s_mov_b32 s60, 0x100001\n"
      ".Lspoll_2015_%=:\n"
      "s_load_dwordx2 s[12:13], s[10:11], 0x0 glc\n"
      "s_load_dwordx2 s[14:15], s[10:11], 0x80 glc\n"
      "s_load_dwordx2 s[16:17], s[10:11], 0x100 glc\n"
      "s_load_dwordx2 s[18:19], s[10:11], 0x180 glc\n"
      ".Lscheck_2016_%=:\n"
      "s_sub_u32 s60, s60, 1\n"
      "s_waitcnt lgkmcnt(0)\n"
      "s_cbranch_scc1 .Lspollend_2017_%=\n"
      "s_cmp_eq_u64 s[12:13], s[36:37]\n"
      "s_cbranch_scc0 .Lspoll_2015_%=\n"
      "s_cmp_eq_u64 s[14:15], s[36:37]\n"
      "s_cbranch_scc0 .Lspoll_2015_%=\n"
      "s_cmp_eq_u64 s[16:17], s[36:37]\n"
      "s_cbranch_scc0 .Lspoll_2015_%=\n"
      "s_cmp_eq_u64 s[18:19], s[36:37]\n"
      "s_cbranch_scc0 .Lspoll_2015_%=\n"
      ".Lspollend_2017_%=:\n"
      "s_barrier\n"
      "buffer_load_dwordx4 v[46:49], v2, s[24:27], s64 offen sc1\n"
      "buffer_load_dwordx4 v[50:53], v2, s[24:27], s64 offen offset:1024 sc1\n"
      "buffer_load_dwordx4 v[76:79], v2, s[24:27], s64 offen offset:2048 sc1\n"
      "buffer_load_dwordx4 v[80:83], v2, s[24:27], s64 offen offset:3072 sc1\n"
      "buffer_load_dwordx4 v[84:87], v2, s[24:27], s65 offen sc1\n"
      "buffer_load_dwordx4 v[88:91], v2, s[24:27], s65 offen offset:1024 sc1\n"
      "buffer_load_dwordx4 v[104:107], v2, s[24:27], s65 offen offset:2048 sc1\n"
      "buffer_load_dwordx4 v[108:111], v2, s[24:27], s65 offen offset:3072 sc1\n"
      "s_waitcnt vmcnt(7)\n"
      "v_mfma_f32_32x32x16_bf16 v[24:39], v[56:59], v[46:49], 0\n"
      "ds_read_b64_tr_b16 v[72:73], v40 offset:4096\n"
      "ds_read_b64_tr_b16 v[74:75], v40 offset:5120\n"
      "v_mfma_f32_32x32x16_bf16 v[8:23], v[60:63], v[46:49], v[8:23]\n"
      "ds_read_b64_tr_b16 v[56:57], v7 offset:4096\n"
      "ds_read_b64_tr_b16 v[58:59], v7 offset:5120\n"
      "buffer_load_dwordx4 v[46:49], v2, s[24:27], s66 offen sc1\n"
      "s_waitcnt vmcnt(7)\n"
      "v_mfma_f32_32x32x16_bf16 v[24:39], v[64:67], v[50:53], v[24:39]\n"
      "ds_read_b64_tr_b16 v[60:61], v40 offset:6144\n"
      "ds_read_b64_tr_b16 v[62:63], v40 offset:7168\n"
      "v_mfma_f32_32x32x16_bf16 v[8:23], v[68:71], v[50:53], v[8:23]\n"
      "ds_read_b64_tr_b16 v[64:65], v7 offset:6144\n"
      "ds_read_b64_tr_b16 v[66:67], v7 offset:7168\n"
      "buffer_load_dwordx4 v[50:53], v2, s[24:27], s66 offen offset:1024 sc1\n"
      "s_waitcnt vmcnt(7) lgkmcnt(6)\n"
      "v_mfma_f32_32x32x16_bf16 v[24:39], v[72:75], v[76:79], v[24:39]\n"
      "ds_read_b64_tr_b16 v[68:69], v40 offset:8192\n"
      "ds_read_b64_tr_b16 v[70:71], v40 offset:9216\n"
      "s_waitcnt lgkmcnt(6)\n"
      "v_mfma_f32_32x32x16_bf16 v[8:23], v[56:59], v[76:79], v[8:23]\n"
      "ds_read_b64_tr_b16 v[72:73], v7 offset:8192\n"
      "ds_read_b64_tr_b16 v[74:75], v7 offset:9216\n"
      "buffer_load_dwordx4 v[76:79], v2, s[24:27], s66 offen offset:2048 sc1\n"
      "s_waitcnt vmcnt(7) lgkmcnt(6)\n"
      "v_mfma_f32_32x32x16_bf16 v[24:39], v[60:63], v[80:83], v[24:39]\n"
      "ds_read_b64_tr_b16 v[56:57], v40 offset:10240\n"
      "ds_read_b64_tr_b16 v[58:59], v40 offset:11264\n"
      "s_waitcnt lgkmcnt(6)\n"
      "v_mfma_f32_32x32x16_bf16 v[8:23], v[64:67], v[80:83], v[8:23]\n"
      "ds_read_b64_tr_b16 v[60:61], v7 offset:10240\n"
      "ds_read_b64_tr_b16 v[62:63], v7 offset:11264\n"
      "buffer_load_dwordx4 v[80:83], v2, s[24:27], s66 offen offset:3072 sc1\n"
      "s_waitcnt vmcnt(7) lgkmcnt(6)\n"
      "v_mfma_f32_32x32x16_bf16 v[24:39], v[68:71], v[84:87], v[24:39]\n"
      "ds_read_b64_tr_b16 v[64:65], v40 offset:12288\n"
      "ds_read_b64_tr_b16 v[66:67], v40 offset:13312\n"
      "s_waitcnt lgkmcnt(6)\n"
      "v_mfma_f32_32x32x16_bf16 v[8:23], v[72:75], v[84:87], v[8:23]\n"
      "ds_read_b64_tr_b16 v[68:69], v7 offset:12288\n"
      "ds_read_b64_tr_b16 v[70:71], v7 offset:13312\n"
      "buffer_load_dwordx4 v[84:87], v2, s[24:27], s67 offen sc1\n"
      "s_waitcnt vmcnt(7) lgkmcnt(6)\n"
      "v_mfma_f32_32x32x16_bf16 v[24:39], v[56:59], v[88:91], v[24:39]\n"
      "ds_read_b64_tr_b16 v[72:73], v40 offset:14336\n"
      "ds_read_b64_tr_b16 v[74:75], v40 offset:15360\n"
      "s_waitcnt lgkmcnt(6)\n"
      "v_mfma_f32_32x32x16_bf16 v[8:23], v[60:63], v[88:91], v[8:23]\n"
      "ds_read_b64_tr_b16 v[56:57], v7 offset:14336\n"
      "ds_read_b64_tr_b16 v[58:59], v7 offset:15360\n"
      "buffer_load_dwordx4 v[88:91], v2, s[24:27], s67 offen offset:1024 sc1\n"
      "s_waitcnt vmcnt(7) lgkmcnt(6)\n"
      "v_mfma_f32_32x32x16_bf16 v[24:39], v[64:67], v[104:107], v[24:39]\n"
      "ds_read_b64_tr_b16 v[60:61], v40 offset:16384\n"
      "ds_read_b64_tr_b16 v[62:63], v40 offset:17408\n"
      "s_waitcnt lgkmcnt(6)\n"
      "v_mfma_f32_32x32x16_bf16 v[8:23], v[68:71], v[104:107], v[8:23]\n"
      "ds_read_b64_tr_b16 v[64:65], v7 offset:16384\n"
      "ds_read_b64_tr_b16 v[66:67], v7 offset:17408\n"
      "buffer_load_dwordx4 v[104:107], v2, s[24:27], s67 offen offset:2048 sc1\n"
      "s_waitcnt vmcnt(7) lgkmcnt(6)\n"
      "v_mfma_f32_32x32x16_bf16 v[24:39], v[72:75], v[108:111], v[24:39]\n"
      "ds_read_b64_tr_b16 v[68:69], v40 offset:18432\n"
      "ds_read_b64_tr_b16 v[70:71], v40 offset:19456\n"
      "s_waitcnt lgkmcnt(6)\n"
      "v_mfma_f32_32x32x16_bf16 v[8:23], v[56:59], v[108:111], v[8:23]\n"
      "ds_read_b64_tr_b16 v[72:73], v7 offset:18432\n"
      "ds_read_b64_tr_b16 v[74:75], v7 offset:19456\n"
      "buffer_load_dwordx4 v[108:111], v2, s[24:27], s67 offen offset:3072 sc1\n"
      "s_waitcnt vmcnt(7) lgkmcnt(6)\n"
      "v_mfma_f32_32x32x16_bf16 v[24:39], v[60:63], v[46:49], v[24:39]\n"
      "ds_read_b64_tr_b16 v[56:57], v40 offset:20480\n"
      "ds_read_b64_tr_b16 v[58:59], v40 offset:21504\n"
      "s_waitcnt lgkmcnt(6)\n"
      "v_mfma_f32_32x32x16_bf16 v[8:23], v[64:67], v[46:49], v[8:23]\n"
      "ds_read_b64_tr_b16 v[60:61], v7 offset:20480\n"
      "ds_read_b64_tr_b16 v[62:63], v7 offset:21504\n"
      "buffer_load_dwordx4 v[46:49], v2, s[24:27], s68 offen sc1\n"
      "s_waitcnt vmcnt(7) lgkmcnt(6)\n"
      "v_mfma_f32_32x32x16_bf16 v[24:39], v[68:71], v[50:53], v[24:39]\n"
      "ds_read_b64_tr_b16 v[64:65], v40 offset:22528\n"
      "ds_read_b64_tr_b16 v[66:67], v40 offset:23552\n"
      "s_waitcnt lgkmcnt(6)\n"
      "v_mfma_f32_32x32x16_bf16 v[8:23], v[72:75], v[50:53], v[8:23]\n"
      "ds_read_b64_tr_b16 v[68:69], v7 offset:22528\n"
      "ds_read_b64_tr_b16 v[70:71], v7 offset:23552\n"
      "buffer_load_dwordx4 v[50:53], v2, s[24:27], s68 offen offset:1024 sc1\n"
      "s_barrier\n"
      "s_waitcnt vmcnt(7) lgkmcnt(6)\n"
      "v_mfma_f32_32x32x16_bf16 v[24:39], v[56:59], v[76:79], v[24:39]\n"
      "ds_read_b64_tr_b16 v[72:73], v44 offset:0\n"
      "ds_read_b64_tr_b16 v[74:75], v44 offset:1024\n"
      "s_waitcnt lgkmcnt(6)\n"
      "v_mfma_f32_32x32x16_bf16 v[8:23], v[60:63], v[76:79], v[8:23]\n"
      "ds_read_b64_tr_b16 v[56:57], v43 offset:0\n"
      "ds_read_b64_tr_b16 v[58:59], v43 offset:1024\n"
      "buffer_load_dwordx4 v[76:79], v2, s[24:27], s68 offen offset:2048 sc1\n"
      "s_waitcnt vmcnt(7) lgkmcnt(6)\n"
      "v_mfma_f32_32x32x16_bf16 v[24:39], v[64:67], v[80:83], v[24:39]\n"
      "ds_read_b64_tr_b16 v[60:61], v44 offset:2048\n"
      "ds_read_b64_tr_b16 v[62:63], v44 offset:3072\n"
      "s_waitcnt lgkmcnt(6)\n"
      "v_mfma_f32_32x32x16_bf16 v[8:23], v[68:71], v[80:83], v[8:23]\n"
      "ds_read_b64_tr_b16 v[64:65], v43 offset:2048\n"
      "ds_read_b64_tr_b16 v[66:67], v43 offset:3072\n"
      "buffer_load_dwordx4 v[80:83], v2, s[24:27], s68 offen offset:3072 sc1\n"
      "s_waitcnt vmcnt(7) lgkmcnt(6)\n"
      "v_mfma_f32_32x32x16_bf16 v[24:39], v[72:75], v[84:87], v[24:39]\n"
      "ds_read_b64_tr_b16 v[68:69], v44 offset:4096\n"
      "ds_read_b64_tr_b16 v[70:71], v44 offset:5120\n"
      "s_waitcnt lgkmcnt(6)\n"
      "v_mfma_f32_32x32x16_bf16 v[8:23], v[56:59], v[84:87], v[8:23]\n"
      "ds_read_b64_tr_b16 v[72:73], v43 offset:4096\n"
      "ds_read_b64_tr_b16 v[74:75], v43 offset:5120\n"
      "buffer_load_dwordx4 v[84:87], v2, s[24:27], s69 offen sc1\n"
      "s_waitcnt vmcnt(7) lgkmcnt(6)\n"
      "v_mfma_f32_32x32x16_bf16 v[24:39], v[60:63], v[88:91], v[24:39]\n"
      "ds_read_b64_tr_b16 v[56:57], v44 offset:6144\n"
      "ds_read_b64_tr_b16 v[58:59], v44 offset:7168\n"
      "s_waitcnt lgkmcnt(6)\n"
      "v_mfma_f32_32x32x16_bf16 v[8:23], v[64:67], v[88:91], v[8:23]\n"
      "ds_read_b64_tr_b16 v[60:61], v43 offset:6144\n"
      "ds_read_b64_tr_b16 v[62:63], v43 offset:7168\n"
      "buffer_load_dwordx4 v[88:91], v2, s[24:27], s69 offen offset:1024 sc1\n"
      "s_waitcnt vmcnt(7) lgkmcnt(6)\n"
      "v_mfma_f32_32x32x16_bf16 v[24:39], v[68:71], v[104:107], v[24:39]\n"
      "ds_read_b64_tr_b16 v[64:65], v44 offset:8192\n"
      "ds_read_b64_tr_b16 v[66:67], v44 offset:9216\n"
      "s_waitcnt lgkmcnt(6)\n"
      "v_mfma_f32_32x32x16_bf16 v[8:23], v[72:75], v[104:107], v[8:23]\n"
      "ds_read_b64_tr_b16 v[68:69], v43 offset:8192\n"
      "ds_read_b64_tr_b16 v[70:71], v43 offset:9216\n"
      "buffer_load_dwordx4 v[104:107], v2, s[24:27], s69 offen offset:2048 sc1\n"
      "s_waitcnt vmcnt(7) lgkmcnt(6)\n"
      "v_mfma_f32_32x32x16_bf16 v[24:39], v[56:59], v[108:111], v[24:39]\n"
      "ds_read_b64_tr_b16 v[72:73], v44 offset:10240\n"
      "ds_read_b64_tr_b16 v[74:75], v44 offset:11264\n"
      "s_waitcnt lgkmcnt(6)\n"
      "v_mfma_f32_32x32x16_bf16 v[8:23], v[60:63], v[108:111], v[8:23]\n"
      "ds_read_b64_tr_b16 v[56:57], v43 offset:10240\n"
      "ds_read_b64_tr_b16 v[58:59], v43 offset:11264\n"
      "buffer_load_dwordx4 v[108:111], v2, s[24:27], s69 offen offset:3072 sc1\n"
      "s_waitcnt vmcnt(7) lgkmcnt(6)\n"
      "v_mfma_f32_32x32x16_bf16 v[24:39], v[64:67], v[46:49], v[24:39]\n"
      "ds_read_b64_tr_b16 v[60:61], v44 offset:12288\n"
      "ds_read_b64_tr_b16 v[62:63], v44 offset:13312\n"
      "s_waitcnt lgkmcnt(6)\n"
      "v_mfma_f32_32x32x16_bf16 v[8:23], v[68:71], v[46:49], v[8:23]\n"
      "ds_read_b64_tr_b16 v[64:65], v43 offset:12288\n"
      "ds_read_b64_tr_b16 v[66:67], v43 offset:13312\n"
      "buffer_load_dwordx4 v[46:49], v2, s[24:27], s70 offen sc1\n"
      "s_waitcnt vmcnt(7) lgkmcnt(6)\n"
      "v_mfma_f32_32x32x16_bf16 v[24:39], v[72:75], v[50:53], v[24:39]\n"
      "ds_read_b64_tr_b16 v[68:69], v44 offset:14336\n"
      "ds_read_b64_tr_b16 v[70:71], v44 offset:15360\n"
      "s_waitcnt lgkmcnt(6)\n"
      "v_mfma_f32_32x32x16_bf16 v[8:23], v[56:59], v[50:53], v[8:23]\n"
      "ds_read_b64_tr_b16 v[72:73], v43 offset:14336\n"
      "ds_read_b64_tr_b16 v[74:75], v43 offset:15360\n"
      "buffer_load_dwordx4 v[50:53], v2, s[24:27], s70 offen offset:1024 sc1\n"
      "s_waitcnt vmcnt(7) lgkmcnt(6)\n"
      "v_mfma_f32_32x32x16_bf16 v[24:39], v[60:63], v[76:79], v[24:39]\n"
      "ds_read_b64_tr_b16 v[56:57], v44 offset:16384\n"
      "ds_read_b64_tr_b16 v[58:59], v44 offset:17408\n"
      "s_waitcnt lgkmcnt(6)\n"
      "v_mfma_f32_32x32x16_bf16 v[8:23], v[64:67], v[76:79], v[8:23]\n"
      "ds_read_b64_tr_b16 v[60:61], v43 offset:16384\n"
      "ds_read_b64_tr_b16 v[62:63], v43 offset:17408\n"
      "buffer_load_dwordx4 v[76:79], v2, s[24:27], s70 offen offset:2048 sc1\n"
      "s_waitcnt vmcnt(7) lgkmcnt(6)\n"
      "v_mfma_f32_32x32x16_bf16 v[24:39], v[68:71], v[80:83], v[24:39]\n"
      "ds_read_b64_tr_b16 v[64:65], v44 offset:18432\n"
      "ds_read_b64_tr_b16 v[66:67], v44 offset:19456\n"
      "s_waitcnt lgkmcnt(6)\n"
      "v_mfma_f32_32x32x16_bf16 v[8:23], v[72:75], v[80:83], v[8:23]\n"
      "ds_read_b64_tr_b16 v[68:69], v43 offset:18432\n"
      "ds_read_b64_tr_b16 v[70:71], v43 offset:19456\n"
      "buffer_load_dwordx4 v[80:83], v2, s[24:27], s70 offen offset:3072 sc1\n"
      "s_waitcnt vmcnt(7) lgkmcnt(6)\n"
      "v_mfma_f32_32x32x16_bf16 v[24:39], v[56:59], v[84:87], v[24:39]\n"
      "ds_read_b64_tr_b16 v[72:73], v44 offset:20480\n"
      "ds_read_b64_tr_b16 v[74:75], v44 offset:21504\n"
      "s_waitcnt lgkmcnt(6)\n"
      "v_mfma_f32_32x32x16_bf16 v[8:23], v[60:63], v[84:87], v[8:23]\n"
      "ds_read_b64_tr_b16 v[56:57], v43 offset:20480\n"
      "ds_read_b64_tr_b16 v[58:59], v43 offset:21504\n"
      "buffer_load_dwordx4 v[84:87], v2, s[24:27], s71 offen sc1\n"
      "s_waitcnt vmcnt(7) lgkmcnt(6)\n"
      "v_mfma_f32_32x32x16_bf16 v[24:39], v[64:67], v[88:91], v[24:39]\n"
      "ds_read_b64_tr_b16 v[60:61], v44 offset:22528\n"
      "ds_read_b64_tr_b16 v[62:63], v44 offset:23552\n"
      "s_waitcnt lgkmcnt(6)\n"
      "v_mfma_f32_32x32x16_bf16 v[8:23], v[68:71], v[88:91], v[8:23]\n"
      "ds_read_b64_tr_b16 v[64:65], v43 offset:22528\n"
      "ds_read_b64_tr_b16 v[66:67], v43 offset:23552\n"
      "buffer_load_dwordx4 v[88:91], v2, s[24:27], s71 offen offset:1024 sc1\n"
      "s_waitcnt vmcnt(7) lgkmcnt(6)\n"
      "v_mfma_f32_32x32x16_bf16 v[24:39], v[72:75], v[104:107], v[24:39]\n"
      "ds_read_b64_tr_b16 v[68:69], v44 offset:24576\n"
      "ds_read_b64_tr_b16 v[70:71], v44 offset:25600\n"
      "s_waitcnt lgkmcnt(6)\n"
      "v_mfma_f32_32x32x16_bf16 v[8:23], v[56:59], v[104:107], v[8:23]\n"
      "ds_read_b64_tr_b16 v[72:73], v43 offset:24576\n"
      "ds_read_b64_tr_b16 v[74:75], v43 offset:25600\n"
      "buffer_load_dwordx4 v[104:107], v2, s[24:27], s71 offen offset:2048 sc1\n"
      "s_waitcnt vmcnt(7) lgkmcnt(6)\n"
      "v_mfma_f32_32x32x16_bf16 v[24:39], v[60:63], v[108:111], v[24:39]\n"
      "ds_read_b64_tr_b16 v[56:57], v44 offset:26624\n"
      "ds_read_b64_tr_b16 v[58:59], v44 offset:27648\n"
      "s_waitcnt lgkmcnt(6)\n"
      "v_mfma_f32_32x32x16_bf16 v[8:23], v[64:67], v[108:111], v[8:23]\n"
      "ds_read_b64_tr_b16 v[60:61], v43 offset:26624\n"
      "ds_read_b64_tr_b16 v[62:63], v43 offset:27648\n"
      "buffer_load_dwordx4 v[108:111], v2, s[24:27], s71 offen offset:3072 sc1\n"
      "s_waitcnt vmcnt(7) lgkmcnt(6)\n"
      "v_mfma_f32_32x32x16_bf16 v[24:39], v[68:71], v[46:49], v[24:39]\n"
      "ds_read_b64_tr_b16 v[64:65], v44 offset:28672\n"
      "ds_read_b64_tr_b16 v[66:67], v44 offset:29696\n"
      "s_waitcnt lgkmcnt(6)\n"
      "v_mfma_f32_32x32x16_bf16 v[8:23], v[72:75], v[46:49], v[8:23]\n"
      "ds_read_b64_tr_b16 v[68:69], v43 offset:28672\n"
      "ds_read_b64_tr_b16 v[70:71], v43 offset:29696\n"
      "buffer_load_dwordx4 v[46:49], v2, s[24:27], s72 offen sc1\n"
      "s_waitcnt vmcnt(7) lgkmcnt(6)\n"
      "v_mfma_f32_32x32x16_bf16 v[24:39], v[56:59], v[50:53], v[24:39]\n"
      "ds_read_b64_tr_b16 v[72:73], v44 offset:30720\n"
      "ds_read_b64_tr_b16 v[74:75], v44 offset:31744\n"
      "s_waitcnt lgkmcnt(6)\n"
      "v_mfma_f32_32x32x16_bf16 v[8:23], v[60:63], v[50:53], v[8:23]\n"
      "ds_read_b64_tr_b16 v[56:57], v43 offset:30720\n"
      "ds_read_b64_tr_b16 v[58:59], v43 offset:31744\n"
      "buffer_load_dwordx4 v[50:53], v2, s[24:27], s72 offen offset:1024 sc1\n"
      "s_waitcnt vmcnt(7) lgkmcnt(6)\n"
      "v_mfma_f32_32x32x16_bf16 v[24:39], v[64:67], v[76:79], v[24:39]\n"
      "ds_read_b64_tr_b16 v[60:61], v44 offset:32768\n"
      "ds_read_b64_tr_b16 v[62:63], v44 offset:33792\n"
      "s_waitcnt lgkmcnt(6)\n"
      "v_mfma_f32_32x32x16_bf16 v[8:23], v[68:71], v[76:79], v[8:23]\n"
      "ds_read_b64_tr_b16 v[64:65], v43 offset:32768\n"
      "ds_read_b64_tr_b16 v[66:67], v43 offset:33792\n"
      "buffer_load_dwordx4 v[76:79], v2, s[24:27], s72 offen offset:2048 sc1\n"
      "s_waitcnt vmcnt(7) lgkmcnt(6)\n"
      "v_mfma_f32_32x32x16_bf16 v[24:39], v[72:75], v[80:83], v[24:39]\n"
      "ds_read_b64_tr_b16 v[68:69], v44 offset:34816\n"
      "ds_read_b64_tr_b16 v[70:71], v44 offset:35840\n"
      "s_waitcnt lgkmcnt(6)\n"
      "v_mfma_f32_32x32x16_bf16 v[8:23], v[56:59], v[80:83], v[8:23]\n"
      "ds_read_b64_tr_b16 v[72:73], v43 offset:34816\n"
      "ds_read_b64_tr_b16 v[74:75], v43 offset:35840\n"
      "buffer_load_dwordx4 v[80:83], v2, s[24:27], s72 offen offset:3072 sc1\n"
      "s_waitcnt vmcnt(7) lgkmcnt(6)\n"
      "v_mfma_f32_32x32x16_bf16 v[24:39], v[60:63], v[84:87], v[24:39]\n"
      "ds_read_b64_tr_b16 v[56:57], v44 offset:36864\n"
      "ds_read_b64_tr_b16 v[58:59], v44 offset:37888\n"
      "s_waitcnt lgkmcnt(6)\n"
      "v_mfma_f32_32x32x16_bf16 v[8:23], v[64:67], v[84:87], v[8:23]\n"
      "ds_read_b64_tr_b16 v[60:61], v43 offset:36864\n"
      "ds_read_b64_tr_b16 v[62:63], v43 offset:37888\n"
      "buffer_load_dwordx4 v[84:87], v2, s[24:27], s73 offen sc1\n"
      "s_waitcnt vmcnt(7) lgkmcnt(6)\n"
      "v_mfma_f32_32x32x16_bf16 v[24:39], v[68:71], v[88:91], v[24:39]\n"
      "ds_read_b64_tr_b16 v[64:65], v44 offset:38912\n"
      "ds_read_b64_tr_b16 v[66:67], v44 offset:39936\n"
      "s_waitcnt lgkmcnt(6)\n"
      "v_mfma_f32_32x32x16_bf16 v[8:23], v[72:75], v[88:91], v[8:23]\n"
      "ds_read_b64_tr_b16 v[68:69], v43 offset:38912\n"
      "ds_read_b64_tr_b16 v[70:71], v43 offset:39936\n"
      "buffer_load_dwordx4 v[88:91], v2, s[24:27], s73 offen offset:1024 sc1\n"
      "s_waitcnt vmcnt(7) lgkmcnt(6)\n"
      "v_mfma_f32_32x32x16_bf16 v[24:39], v[56:59], v[104:107], v[24:39]\n"
      "ds_read_b64_tr_b16 v[72:73], v44 offset:40960\n"
      "ds_read_b64_tr_b16 v[74:75], v44 offset:41984\n"
      "s_waitcnt lgkmcnt(6)\n"
      "v_mfma_f32_32x32x16_bf16 v[8:23], v[60:63], v[104:107], v[8:23]\n"
      "ds_read_b64_tr_b16 v[56:57], v43 offset:40960\n"
      "ds_read_b64_tr_b16 v[58:59], v43 offset:41984\n"
      "buffer_load_dwordx4 v[104:107], v2, s[24:27], s73 offen offset:2048 sc1\n"
      "s_waitcnt vmcnt(7) lgkmcnt(6)\n"
      "v_mfma_f32_32x32x16_bf16 v[24:39], v[64:67], v[108:111], v[24:39]\n"
      "ds_read_b64_tr_b16 v[60:61], v44 offset:43008\n"
      "ds_read_b64_tr_b16 v[62:63], v44 offset:44032\n"
      "s_waitcnt lgkmcnt(6)\n"
      "v_mfma_f32_32x32x16_bf16 v[8:23], v[68:71], v[108:111], v[8:23]\n"
      "ds_read_b64_tr_b16 v[64:65], v43 offset:43008\n"
      "ds_read_b64_tr_b16 v[66:67], v43 offset:44032\n"
      "buffer_load_dwordx4 v[108:111], v2, s[24:27], s73 offen offset:3072 sc1\n"
      "s_waitcnt vmcnt(7) lgkmcnt(6)\n"
      "v_mfma_f32_32x32x16_bf16 v[24:39], v[72:75], v[46:49], v[24:39]\n"
      "ds_read_b64_tr_b16 v[68:69], v44 offset:45056\n"
      "ds_read_b64_tr_b16 v[70:71], v44 offset:46080\n"
      "s_waitcnt lgkmcnt(6)\n"
      "v_mfma_f32_32x32x16_bf16 v[8:23], v[56:59], v[46:49], v[8:23]\n"
      "ds_read_b64_tr_b16 v[72:73], v43 offset:45056\n"
      "ds_read_b64_tr_b16 v[74:75], v43 offset:46080\n"
      "buffer_load_dwordx4 v[46:49], v2, s[24:27], s74 offen sc1\n"
      "s_waitcnt vmcnt(7) lgkmcnt(6)\n"
      "v_mfma_f32_32x32x16_bf16 v[24:39], v[60:63], v[50:53], v[24:39]\n"
      "ds_read_b64_tr_b16 v[56:57], v44 offset:47104\n"
      "ds_read_b64_tr_b16 v[58:59], v44 offset:48128\n"
      "s_waitcnt lgkmcnt(6)\n"
      "v_mfma_f32_32x32x16_bf16 v[8:23], v[64:67], v[50:53], v[8:23]\n"
      "ds_read_b64_tr_b16 v[60:61], v43 offset:47104\n"
      "ds_read_b64_tr_b16 v[62:63], v43 offset:48128\n"
      "buffer_load_dwordx4 v[50:53], v2, s[24:27], s74 offen offset:1024 sc1\n"
      "s_barrier\n"
      "s_waitcnt vmcnt(7) lgkmcnt(6)\n"
      "v_mfma_f32_32x32x16_bf16 v[24:39], v[68:71], v[76:79], v[24:39]\n"
      "ds_read_b64_tr_b16 v[64:65], v40 offset:0\n"
      "ds_read_b64_tr_b16 v[66:67], v40 offset:1024\n"
      "s_waitcnt lgkmcnt(6)\n"
      "v_mfma_f32_32x32x16_bf16 v[8:23], v[72:75], v[76:79], v[8:23]\n"
      "ds_read_b64_tr_b16 v[68:69], v7 offset:0\n"
      "ds_read_b64_tr_b16 v[70:71], v7 offset:1024\n"
      "buffer_load_dwordx4 v[76:79], v2, s[24:27], s74 offen offset:2048 sc1\n"
      "s_waitcnt vmcnt(7) lgkmcnt(6)\n"
      "v_mfma_f32_32x32x16_bf16 v[24:39], v[56:59], v[80:83], v[24:39]\n"
      "ds_read_b64_tr_b16 v[72:73], v40 offset:2048\n"
      "ds_read_b64_tr_b16 v[74:75], v40 offset:3072\n"
      "s_waitcnt lgkmcnt(6)\n"
      "v_mfma_f32_32x32x16_bf16 v[8:23], v[60:63], v[80:83], v[8:23]\n"
      "ds_read_b64_tr_b16 v[56:57], v7 offset:2048\n"
      "ds_read_b64_tr_b16 v[58:59], v7 offset:3072\n"
      "s_waitcnt vmcnt(6) lgkmcnt(6)\n"
      "v_mfma_f32_32x32x16_bf16 v[24:39], v[64:67], v[84:87], v[24:39]\n"
      "ds_read_b64_tr_b16 v[60:61], v40 offset:4096\n"
      "ds_read_b64_tr_b16 v[62:63], v40 offset:5120\n"
      "s_waitcnt lgkmcnt(6)\n"
      "v_mfma_f32_32x32x16_bf16 v[8:23], v[68:71], v[84:87], v[8:23]\n"
      "ds_read_b64_tr_b16 v[64:65], v7 offset:4096\n"
      "ds_read_b64_tr_b16 v[66:67], v7 offset:5120\n"
      "s_waitcnt vmcnt(5) lgkmcnt(6)\n"
      "v_mfma_f32_32x32x16_bf16 v[24:39], v[72:75], v[88:91], v[24:39]\n"
      "ds_read_b64_tr_b16 v[68:69], v40 offset:6144\n"
      "ds_read_b64_tr_b16 v[70:71], v40 offset:7168\n"
      "s_waitcnt lgkmcnt(6)\n"
      "v_mfma_f32_32x32x16_bf16 v[8:23], v[56:59], v[88:91], v[8:23]\n"
      "ds_read_b64_tr_b16 v[72:73], v7 offset:6144\n"
      "ds_read_b64_tr_b16 v[74:75], v7 offset:7168\n"
      "s_waitcnt vmcnt(4) lgkmcnt(6)\n"
      "v_mfma_f32_32x32x16_bf16 v[24:39], v[60:63], v[104:107], v[24:39]\n"
      "ds_read_b64_tr_b16 v[56:57], v40 offset:8192\n"
      "ds_read_b64_tr_b16 v[58:59], v40 offset:9216\n"
      "s_waitcnt lgkmcnt(6)\n"
      "v_mfma_f32_32x32x16_bf16 v[8:23], v[64:67], v[104:107], v[8:23]\n"
      "ds_read_b64_tr_b16 v[60:61], v7 offset:8192\n"
      "ds_read_b64_tr_b16 v[62:63], v7 offset:9216\n"
      "s_waitcnt vmcnt(3) lgkmcnt(6)\n"
      "v_mfma_f32_32x32x16_bf16 v[24:39], v[68:71], v[108:111], v[24:39]\n"
      "ds_read_b64_tr_b16 v[64:65], v40 offset:10240\n"
      "ds_read_b64_tr_b16 v[66:67], v40 offset:11264\n"
      "s_waitcnt lgkmcnt(6)\n"
      "v_mfma_f32_32x32x16_bf16 v[8:23], v[72:75], v[108:111], v[8:23]\n"
      "ds_read_b64_tr_b16 v[68:69], v7 offset:10240\n"
      "ds_read_b64_tr_b16 v[70:71], v7 offset:11264\n"
      "s_waitcnt vmcnt(2) lgkmcnt(6)\n"
      "v_mfma_f32_32x32x16_bf16 v[24:39], v[56:59], v[46:49], v[24:39]\n"
      "ds_read_b64_tr_b16 v[72:73], v40 offset:12288\n"
      "ds_read_b64_tr_b16 v[74:75], v40 offset:13312\n"
      "s_waitcnt lgkmcnt(6)\n"
      "v_mfma_f32_32x32x16_bf16 v[8:23], v[60:63], v[46:49], v[8:23]\n"
      "s_nop 0\n"
      ".Lpage8_%=:\n"
      "s_cbranch_execz .Lret_%=\n"
      "ds_read_b64_tr_b16 v[56:57], v7 offset:12288\n"
      "ds_read_b64_tr_b16 v[58:59], v7 offset:13312\n"
      "s_waitcnt vmcnt(1) lgkmcnt(6)\n"
      "v_mfma_f32_32x32x16_bf16 v[24:39], v[64:67], v[50:53], v[24:39]\n"
      "s_waitcnt lgkmcnt(4)\n"
      "v_mfma_f32_32x32x16_bf16 v[8:23], v[68:71], v[50:53], v[8:23]\n"
      "s_waitcnt vmcnt(0) lgkmcnt(2)\n"
      "v_mfma_f32_32x32x16_bf16 v[24:39], v[72:75], v[76:79], v[24:39]\n"
      "s_waitcnt lgkmcnt(0)\n"
      "v_mfma_f32_32x32x16_bf16 v[8:23], v[56:59], v[76:79], v[8:23]\n"
      "s_nop 9\n"
      "ds_write_b128 v41, v[24:27] offset:0\n"
      "ds_write_b128 v41, v[28:31] offset:1024\n"
      "ds_write_b128 v41, v[32:35] offset:2048\n"
      "ds_write_b128 v41, v[36:39] offset:3072\n"
      "s_waitcnt lgkmcnt(0)\n"
      "s_barrier\n"
      "ds_read_b128 v[24:27], v42 offset:0\n"
      "ds_read_b128 v[28:31], v42 offset:1024\n"
      "ds_read_b128 v[32:35], v42 offset:2048\n"
      "ds_read_b128 v[36:39], v42 offset:3072\n"
      "s_waitcnt lgkmcnt(3)\n"
      "v_pk_add_f32 v[8:9], v[8:9], v[24:25]\n"
      "v_pk_add_f32 v[10:11], v[10:11], v[26:27]\n"
      "s_waitcnt lgkmcnt(2)\n"
      "v_pk_add_f32 v[12:13], v[12:13], v[28:29]\n"
      "v_pk_add_f32 v[14:15], v[14:15], v[30:31]\n"
      "v_pk_mul_f32 v[8:9], v[8:9], v[152:153] op_sel_hi:[1,0]\n"
      "v_pk_mul_f32 v[10:11], v[10:11], v[152:153] op_sel_hi:[1,0]\n"
      "v_pk_mul_f32 v[12:13], v[12:13], v[152:153] op_sel_hi:[1,0]\n"
      "v_pk_mul_f32 v[14:15], v[14:15], v[152:153] op_sel_hi:[1,0]\n"
      "v_cvt_pk_bf16_f32 v56, v8, v9\n"
      "v_cvt_pk_bf16_f32 v57, v10, v11\n"
      "v_cvt_pk_bf16_f32 v58, v12, v13\n"
      "v_cvt_pk_bf16_f32 v59, v14, v15\n"
      "s_nop 0\n"
      "v_permlane32_swap_b32 v56, v58\n"
      "v_permlane32_swap_b32 v57, v59\n"
      "buffer_store_dwordx4 v[56:59], v45, s[28:31], s62 offen\n"
      "s_waitcnt lgkmcnt(1)\n"
      "v_pk_add_f32 v[16:17], v[16:17], v[32:33]\n"
      "v_pk_add_f32 v[18:19], v[18:19], v[34:35]\n"
      "s_waitcnt lgkmcnt(0)\n"
      "v_pk_add_f32 v[20:21], v[20:21], v[36:37]\n"
      "v_pk_add_f32 v[22:23], v[22:23], v[38:39]\n"
      "v_pk_mul_f32 v[16:17], v[16:17], v[152:153] op_sel_hi:[1,0]\n"
      "v_pk_mul_f32 v[18:19], v[18:19], v[152:153] op_sel_hi:[1,0]\n"
      "v_pk_mul_f32 v[20:21], v[20:21], v[152:153] op_sel_hi:[1,0]\n"
      "v_pk_mul_f32 v[22:23], v[22:23], v[152:153] op_sel_hi:[1,0]\n"
      "v_cvt_pk_bf16_f32 v60, v16, v17\n"
      "v_cvt_pk_bf16_f32 v61, v18, v19\n"
      "v_cvt_pk_bf16_f32 v62, v20, v21\n"
      "v_cvt_pk_bf16_f32 v63, v22, v23\n"
      "s_nop 0\n"
      "v_permlane32_swap_b32 v60, v62\n"
      "v_permlane32_swap_b32 v61, v63\n"
      "buffer_store_dwordx4 v[60:63], v45, s[28:31], s62 offen offset:32\n"
      "s_endpgm\n"
      ".Lkend_%=:\n"
      ".Lslow_2005_%=:\n"
      "s_nop 11\n"
      "s_waitcnt lgkmcnt(0)\n"
      "v_lshlrev_b32 v218, 2, v6\n"
      "v_sub_u32 v218, v5, v218\n"
      "v_mov_b32 v219, 0xf149f2ca\n"
      "v_mov_b32 v214, 0xf149f2ca\n"
      "s_lshl_b32 s59, s39, 1\n"
      "s_add_u32 s59, s59, 1\n"
      "s_min_u32 s59, s59, 8\n"
      "s_mov_b32 s58, 0\n"
      "v_add_u32 v215, 8192, v74\n"
      "v_mov_b32 v216, v87\n"
      "s_nop 1\n"
      ".Lslowloop_2507_%=:\n"
      "ds_read_b128 v[222:225], v215 offset:0\n"
      "s_cmp_eq_u32 s58, 0\n"
      "s_cselect_b32 s57, 0, 64\n"
      "s_cmp_eq_u32 s58, 8\n"
      "s_cselect_b32 s56, 0, -64\n"
      "s_waitcnt lgkmcnt(0)\n"
      "v_mfma_f32_32x32x16_bf16 v[198:213], v[222:225], v[44:47], 0\n"
      "v_sub_u32 v220, 0, v218\n"
      "v_cmp_lt_i32 s[12:13], s56, v220\n"
      "v_cmp_ge_i32 vcc, s57, v220\n"
      "s_nop 1\n"
      "s_and_b64 vcc, vcc, s[12:13]\n"
      "s_nop 5\n"
      "v_cndmask_b32 v198, v219, v198, vcc\n"
      "v_max_f32 v214, v214, v198\n"
      "v_sub_u32 v220, 1, v218\n"
      "v_cmp_lt_i32 s[12:13], s56, v220\n"
      "v_cmp_ge_i32 vcc, s57, v220\n"
      "s_nop 1\n"
      "s_and_b64 vcc, vcc, s[12:13]\n"
      "v_cndmask_b32 v199, v219, v199, vcc\n"
      "v_max_f32 v214, v214, v199\n"
      "v_sub_u32 v220, 2, v218\n"
      "v_cmp_lt_i32 s[12:13], s56, v220\n"
      "v_cmp_ge_i32 vcc, s57, v220\n"
      "s_nop 1\n"
      "s_and_b64 vcc, vcc, s[12:13]\n"
      "v_cndmask_b32 v200, v219, v200, vcc\n"
      "v_max_f32 v214, v214, v200\n"
      "v_sub_u32 v220, 3, v218\n"
      "v_cmp_lt_i32 s[12:13], s56, v220\n"
      "v_cmp_ge_i32 vcc, s57, v220\n"
      "s_nop 1\n"
      "s_and_b64 vcc, vcc, s[12:13]\n"
      "v_cndmask_b32 v201, v219, v201, vcc\n"
      "v_max_f32 v214, v214, v201\n"
      "v_sub_u32 v220, 8, v218\n"
      "v_cmp_lt_i32 s[12:13], s56, v220\n"
      "v_cmp_ge_i32 vcc, s57, v220\n"
      "s_nop 1\n"
      "s_and_b64 vcc, vcc, s[12:13]\n"
      "v_cndmask_b32 v202, v219, v202, vcc\n"
      "v_max_f32 v214, v214, v202\n"
      "v_sub_u32 v220, 9, v218\n"
      "v_cmp_lt_i32 s[12:13], s56, v220\n"
      "v_cmp_ge_i32 vcc, s57, v220\n"
      "s_nop 1\n"
      "s_and_b64 vcc, vcc, s[12:13]\n"
      "v_cndmask_b32 v203, v219, v203, vcc\n"
      "v_max_f32 v214, v214, v203\n"
      "v_sub_u32 v220, 10, v218\n"
      "v_cmp_lt_i32 s[12:13], s56, v220\n"
      "v_cmp_ge_i32 vcc, s57, v220\n"
      "s_nop 1\n"
      "s_and_b64 vcc, vcc, s[12:13]\n"
      "v_cndmask_b32 v204, v219, v204, vcc\n"
      "v_max_f32 v214, v214, v204\n"
      "v_sub_u32 v220, 11, v218\n"
      "v_cmp_lt_i32 s[12:13], s56, v220\n"
      "v_cmp_ge_i32 vcc, s57, v220\n"
      "s_nop 1\n"
      "s_and_b64 vcc, vcc, s[12:13]\n"
      "v_cndmask_b32 v205, v219, v205, vcc\n"
      "v_max_f32 v214, v214, v205\n"
      "v_sub_u32 v220, 16, v218\n"
      "v_cmp_lt_i32 s[12:13], s56, v220\n"
      "v_cmp_ge_i32 vcc, s57, v220\n"
      "s_nop 1\n"
      "s_and_b64 vcc, vcc, s[12:13]\n"
      "v_cndmask_b32 v206, v219, v206, vcc\n"
      "v_max_f32 v214, v214, v206\n"
      "v_sub_u32 v220, 17, v218\n"
      "v_cmp_lt_i32 s[12:13], s56, v220\n"
      "v_cmp_ge_i32 vcc, s57, v220\n"
      "s_nop 1\n"
      "s_and_b64 vcc, vcc, s[12:13]\n"
      "v_cndmask_b32 v207, v219, v207, vcc\n"
      "v_max_f32 v214, v214, v207\n"
      "v_sub_u32 v220, 18, v218\n"
      "v_cmp_lt_i32 s[12:13], s56, v220\n"
      "v_cmp_ge_i32 vcc, s57, v220\n"
      "s_nop 1\n"
      "s_and_b64 vcc, vcc, s[12:13]\n"
      "v_cndmask_b32 v208, v219, v208, vcc\n"
      "v_max_f32 v214, v214, v208\n"
      "v_sub_u32 v220, 19, v218\n"
      "v_cmp_lt_i32 s[12:13], s56, v220\n"
      "v_cmp_ge_i32 vcc, s57, v220\n"
      "s_nop 1\n"
      "s_and_b64 vcc, vcc, s[12:13]\n"
      "v_cndmask_b32 v209, v219, v209, vcc\n"
      "v_max_f32 v214, v214, v209\n"
      "v_sub_u32 v220, 24, v218\n"
      "v_cmp_lt_i32 s[12:13], s56, v220\n"
      "v_cmp_ge_i32 vcc, s57, v220\n"
      "s_nop 1\n"
      "s_and_b64 vcc, vcc, s[12:13]\n"
      "v_cndmask_b32 v210, v219, v210, vcc\n"
      "v_max_f32 v214, v214, v210\n"
      "v_sub_u32 v220, 25, v218\n"
      "v_cmp_lt_i32 s[12:13], s56, v220\n"
      "v_cmp_ge_i32 vcc, s57, v220\n"
      "s_nop 1\n"
      "s_and_b64 vcc, vcc, s[12:13]\n"
      "v_cndmask_b32 v211, v219, v211, vcc\n"
      "v_max_f32 v214, v214, v211\n"
      "v_sub_u32 v220, 26, v218\n"
      "v_cmp_lt_i32 s[12:13], s56, v220\n"
      "v_cmp_ge_i32 vcc, s57, v220\n"
      "s_nop 1\n"
      "s_and_b64 vcc, vcc, s[12:13]\n"
      "v_cndmask_b32 v212, v219, v212, vcc\n"
      "v_max_f32 v214, v214, v212\n"
      "v_sub_u32 v220, 27, v218\n"
      "v_cmp_lt_i32 s[12:13], s56, v220\n"
      "v_cmp_ge_i32 vcc, s57, v220\n"
      "s_nop 1\n"
      "s_and_b64 vcc, vcc, s[12:13]\n"
      "v_cndmask_b32 v213, v219, v213, vcc\n"
      "v_max_f32 v214, v214, v213\n"
      "v_subrev_u32 v215, 0x2000, v215\n"
      "v_sub_u32 v216, v216, v77\n"
      "s_add_u32 s58, s58, 1\n"
      "s_cmp_le_u32 s58, s59\n"
      "s_cbranch_scc1 .Lslowloop_2507_%=\n"
      "v_mov_b32 v220, v214\n"
      "s_nop 1\n"
      "v_permlane32_swap_b32 v214, v220\n"
      "v_max_f32 v214, v214, v220\n"
      "s_mov_b32 s58, 0\n"
      "v_add_u32 v215, 8192, v74\n"
      "v_mov_b32 v216, v87\n"
      "v_mov_b32 v146, 0\n"
      "v_mov_b32 v147, 0\n"
      "v_mov_b32 v148, 0\n"
      "v_mov_b32 v149, 0\n"
      "v_mov_b32 v150, 0\n"
      "v_mov_b32 v151, 0\n"
      "v_mov_b32 v152, 0\n"
      "v_mov_b32 v153, 0\n"
      "v_mov_b32 v154, 0\n"
      "v_mov_b32 v155, 0\n"
      "v_mov_b32 v156, 0\n"
      "v_mov_b32 v157, 0\n"
      "v_mov_b32 v158, 0\n"
      "v_mov_b32 v159, 0\n"
      "v_mov_b32 v160, 0\n"
      "v_mov_b32 v161, 0\n"
      "s_nop 1\n"
      ".Lslowloop_2508_%=:\n"
      "ds_read_b128 v[222:225], v215 offset:0\n"
      "ds_read_b128 v[226:229], v216 offset:0\n"
      "ds_read_b128 v[230:233], v216 offset:1024\n"
      "s_cmp_eq_u32 s58, 0\n"
      "s_cselect_b32 s57, 0, 64\n"
      "s_cmp_eq_u32 s58, 8\n"
      "s_cselect_b32 s56, 0, -64\n"
      "s_waitcnt lgkmcnt(2)\n"
      "v_mfma_f32_32x32x16_bf16 v[198:213], v[222:225], v[44:47], 0\n"
      "v_sub_u32 v220, 0, v218\n"
      "v_cmp_lt_i32 s[12:13], s56, v220\n"
      "v_cmp_ge_i32 vcc, s57, v220\n"
      "s_nop 1\n"
      "s_and_b64 vcc, vcc, s[12:13]\n"
      "s_nop 5\n"
      "v_cndmask_b32 v198, v219, v198, vcc\n"
      "v_sub_f32 v198, v198, v214\n"
      "v_exp_f32 v198, v198\n"
      "s_nop 0\n"
      "v_cndmask_b32 v198, 0, v198, vcc\n"
      "v_sub_u32 v220, 1, v218\n"
      "v_cmp_lt_i32 s[12:13], s56, v220\n"
      "v_cmp_ge_i32 vcc, s57, v220\n"
      "s_nop 1\n"
      "s_and_b64 vcc, vcc, s[12:13]\n"
      "v_cndmask_b32 v199, v219, v199, vcc\n"
      "v_sub_f32 v199, v199, v214\n"
      "v_exp_f32 v199, v199\n"
      "s_nop 0\n"
      "v_cndmask_b32 v199, 0, v199, vcc\n"
      "v_sub_u32 v220, 2, v218\n"
      "v_cmp_lt_i32 s[12:13], s56, v220\n"
      "v_cmp_ge_i32 vcc, s57, v220\n"
      "s_nop 1\n"
      "s_and_b64 vcc, vcc, s[12:13]\n"
      "v_cndmask_b32 v200, v219, v200, vcc\n"
      "v_sub_f32 v200, v200, v214\n"
      "v_exp_f32 v200, v200\n"
      "s_nop 0\n"
      "v_cndmask_b32 v200, 0, v200, vcc\n"
      "v_sub_u32 v220, 3, v218\n"
      "v_cmp_lt_i32 s[12:13], s56, v220\n"
      "v_cmp_ge_i32 vcc, s57, v220\n"
      "s_nop 1\n"
      "s_and_b64 vcc, vcc, s[12:13]\n"
      "v_cndmask_b32 v201, v219, v201, vcc\n"
      "v_sub_f32 v201, v201, v214\n"
      "v_exp_f32 v201, v201\n"
      "s_nop 0\n"
      "v_cndmask_b32 v201, 0, v201, vcc\n"
      "v_sub_u32 v220, 8, v218\n"
      "v_cmp_lt_i32 s[12:13], s56, v220\n"
      "v_cmp_ge_i32 vcc, s57, v220\n"
      "s_nop 1\n"
      "s_and_b64 vcc, vcc, s[12:13]\n"
      "v_cndmask_b32 v202, v219, v202, vcc\n"
      "v_sub_f32 v202, v202, v214\n"
      "v_exp_f32 v202, v202\n"
      "s_nop 0\n"
      "v_cndmask_b32 v202, 0, v202, vcc\n"
      "v_sub_u32 v220, 9, v218\n"
      "v_cmp_lt_i32 s[12:13], s56, v220\n"
      "v_cmp_ge_i32 vcc, s57, v220\n"
      "s_nop 1\n"
      "s_and_b64 vcc, vcc, s[12:13]\n"
      "v_cndmask_b32 v203, v219, v203, vcc\n"
      "v_sub_f32 v203, v203, v214\n"
      "v_exp_f32 v203, v203\n"
      "s_nop 0\n"
      "v_cndmask_b32 v203, 0, v203, vcc\n"
      "v_sub_u32 v220, 10, v218\n"
      "v_cmp_lt_i32 s[12:13], s56, v220\n"
      "v_cmp_ge_i32 vcc, s57, v220\n"
      "s_nop 1\n"
      "s_and_b64 vcc, vcc, s[12:13]\n"
      "v_cndmask_b32 v204, v219, v204, vcc\n"
      "v_sub_f32 v204, v204, v214\n"
      "v_exp_f32 v204, v204\n"
      "s_nop 0\n"
      "v_cndmask_b32 v204, 0, v204, vcc\n"
      "v_sub_u32 v220, 11, v218\n"
      "v_cmp_lt_i32 s[12:13], s56, v220\n"
      "v_cmp_ge_i32 vcc, s57, v220\n"
      "s_nop 1\n"
      "s_and_b64 vcc, vcc, s[12:13]\n"
      "v_cndmask_b32 v205, v219, v205, vcc\n"
      "v_sub_f32 v205, v205, v214\n"
      "v_exp_f32 v205, v205\n"
      "s_nop 0\n"
      "v_cndmask_b32 v205, 0, v205, vcc\n"
      "v_sub_u32 v220, 16, v218\n"
      "v_cmp_lt_i32 s[12:13], s56, v220\n"
      "v_cmp_ge_i32 vcc, s57, v220\n"
      "s_nop 1\n"
      "s_and_b64 vcc, vcc, s[12:13]\n"
      "v_cndmask_b32 v206, v219, v206, vcc\n"
      "v_sub_f32 v206, v206, v214\n"
      "v_exp_f32 v206, v206\n"
      "s_nop 0\n"
      "v_cndmask_b32 v206, 0, v206, vcc\n"
      "v_sub_u32 v220, 17, v218\n"
      "v_cmp_lt_i32 s[12:13], s56, v220\n"
      "v_cmp_ge_i32 vcc, s57, v220\n"
      "s_nop 1\n"
      "s_and_b64 vcc, vcc, s[12:13]\n"
      "v_cndmask_b32 v207, v219, v207, vcc\n"
      "v_sub_f32 v207, v207, v214\n"
      "v_exp_f32 v207, v207\n"
      "s_nop 0\n"
      "v_cndmask_b32 v207, 0, v207, vcc\n"
      "v_sub_u32 v220, 18, v218\n"
      "v_cmp_lt_i32 s[12:13], s56, v220\n"
      "v_cmp_ge_i32 vcc, s57, v220\n"
      "s_nop 1\n"
      "s_and_b64 vcc, vcc, s[12:13]\n"
      "v_cndmask_b32 v208, v219, v208, vcc\n"
      "v_sub_f32 v208, v208, v214\n"
      "v_exp_f32 v208, v208\n"
      "s_nop 0\n"
      "v_cndmask_b32 v208, 0, v208, vcc\n"
      "v_sub_u32 v220, 19, v218\n"
      "v_cmp_lt_i32 s[12:13], s56, v220\n"
      "v_cmp_ge_i32 vcc, s57, v220\n"
      "s_nop 1\n"
      "s_and_b64 vcc, vcc, s[12:13]\n"
      "v_cndmask_b32 v209, v219, v209, vcc\n"
      "v_sub_f32 v209, v209, v214\n"
      "v_exp_f32 v209, v209\n"
      "s_nop 0\n"
      "v_cndmask_b32 v209, 0, v209, vcc\n"
      "v_sub_u32 v220, 24, v218\n"
      "v_cmp_lt_i32 s[12:13], s56, v220\n"
      "v_cmp_ge_i32 vcc, s57, v220\n"
      "s_nop 1\n"
      "s_and_b64 vcc, vcc, s[12:13]\n"
      "v_cndmask_b32 v210, v219, v210, vcc\n"
      "v_sub_f32 v210, v210, v214\n"
      "v_exp_f32 v210, v210\n"
      "s_nop 0\n"
      "v_cndmask_b32 v210, 0, v210, vcc\n"
      "v_sub_u32 v220, 25, v218\n"
      "v_cmp_lt_i32 s[12:13], s56, v220\n"
      "v_cmp_ge_i32 vcc, s57, v220\n"
      "s_nop 1\n"
      "s_and_b64 vcc, vcc, s[12:13]\n"
      "v_cndmask_b32 v211, v219, v211, vcc\n"
      "v_sub_f32 v211, v211, v214\n"
      "v_exp_f32 v211, v211\n"
      "s_nop 0\n"
      "v_cndmask_b32 v211, 0, v211, vcc\n"
      "v_sub_u32 v220, 26, v218\n"
      "v_cmp_lt_i32 s[12:13], s56, v220\n"
      "v_cmp_ge_i32 vcc, s57, v220\n"
      "s_nop 1\n"
      "s_and_b64 vcc, vcc, s[12:13]\n"
      "v_cndmask_b32 v212, v219, v212, vcc\n"
      "v_sub_f32 v212, v212, v214\n"
      "v_exp_f32 v212, v212\n"
      "s_nop 0\n"
      "v_cndmask_b32 v212, 0, v212, vcc\n"
      "v_sub_u32 v220, 27, v218\n"
      "v_cmp_lt_i32 s[12:13], s56, v220\n"
      "v_cmp_ge_i32 vcc, s57, v220\n"
      "s_nop 1\n"
      "s_and_b64 vcc, vcc, s[12:13]\n"
      "v_cndmask_b32 v213, v219, v213, vcc\n"
      "v_sub_f32 v213, v213, v214\n"
      "v_exp_f32 v213, v213\n"
      "s_nop 0\n"
      "v_cndmask_b32 v213, 0, v213, vcc\n"
      "v_cvt_pk_bf16_f32 v234, v198, v199\n"
      "v_cvt_pk_bf16_f32 v235, v200, v201\n"
      "v_cvt_pk_bf16_f32 v236, v202, v203\n"
      "v_cvt_pk_bf16_f32 v237, v204, v205\n"
      "v_cvt_pk_bf16_f32 v238, v206, v207\n"
      "v_cvt_pk_bf16_f32 v239, v208, v209\n"
      "v_cvt_pk_bf16_f32 v240, v210, v211\n"
      "v_cvt_pk_bf16_f32 v241, v212, v213\n"
      "s_waitcnt lgkmcnt(1)\n"
      "v_mfma_f32_32x32x16_bf16 v[146:161], v[226:229], v[234:237], v[146:161]\n"
      "s_waitcnt lgkmcnt(0)\n"
      "v_mfma_f32_32x32x16_bf16 v[146:161], v[230:233], v[238:241], v[146:161]\n"
      "v_subrev_u32 v215, 0x2000, v215\n"
      "v_sub_u32 v216, v216, v77\n"
      "s_add_u32 s58, s58, 1\n"
      "s_cmp_le_u32 s58, s59\n"
      "s_nop 7\n"
      "s_cbranch_scc1 .Lslowloop_2508_%=\n"
      "v_rcp_f32 v188, v154\n"
      "s_nop 1\n"
      "s_branch .Lback_2006_%=\n"
      ".Lslow_2007_%=:\n"
      "s_nop 11\n"
      "s_waitcnt lgkmcnt(0)\n"
      "v_lshlrev_b32 v218, 2, v6\n"
      "v_sub_u32 v218, v5, v218\n"
      "v_mov_b32 v219, 0xf149f2ca\n"
      "v_mov_b32 v214, 0xf149f2ca\n"
      "s_lshl_b32 s59, s39, 1\n"
      "s_add_u32 s59, s59, 0\n"
      "s_min_u32 s59, s59, 8\n"
      "s_mov_b32 s58, 0\n"
      "v_add_u32 v215, 0, v74\n"
      "v_mov_b32 v216, v86\n"
      "s_nop 1\n"
      ".Lslowloop_2509_%=:\n"
      "ds_read_b128 v[222:225], v215 offset:0\n"
      "s_cmp_eq_u32 s58, 0\n"
      "s_cselect_b32 s57, 0, 64\n"
      "s_cmp_eq_u32 s58, 8\n"
      "s_cselect_b32 s56, 0, -64\n"
      "s_waitcnt lgkmcnt(0)\n"
      "v_mfma_f32_32x32x16_bf16 v[198:213], v[222:225], v[8:11], 0\n"
      "v_sub_u32 v220, 0, v218\n"
      "v_cmp_lt_i32 s[12:13], s56, v220\n"
      "v_cmp_ge_i32 vcc, s57, v220\n"
      "s_nop 1\n"
      "s_and_b64 vcc, vcc, s[12:13]\n"
      "s_nop 5\n"
      "v_cndmask_b32 v198, v219, v198, vcc\n"
      "v_max_f32 v214, v214, v198\n"
      "v_sub_u32 v220, 1, v218\n"
      "v_cmp_lt_i32 s[12:13], s56, v220\n"
      "v_cmp_ge_i32 vcc, s57, v220\n"
      "s_nop 1\n"
      "s_and_b64 vcc, vcc, s[12:13]\n"
      "v_cndmask_b32 v199, v219, v199, vcc\n"
      "v_max_f32 v214, v214, v199\n"
      "v_sub_u32 v220, 2, v218\n"
      "v_cmp_lt_i32 s[12:13], s56, v220\n"
      "v_cmp_ge_i32 vcc, s57, v220\n"
      "s_nop 1\n"
      "s_and_b64 vcc, vcc, s[12:13]\n"
      "v_cndmask_b32 v200, v219, v200, vcc\n"
      "v_max_f32 v214, v214, v200\n"
      "v_sub_u32 v220, 3, v218\n"
      "v_cmp_lt_i32 s[12:13], s56, v220\n"
      "v_cmp_ge_i32 vcc, s57, v220\n"
      "s_nop 1\n"
      "s_and_b64 vcc, vcc, s[12:13]\n"
      "v_cndmask_b32 v201, v219, v201, vcc\n"
      "v_max_f32 v214, v214, v201\n"
      "v_sub_u32 v220, 8, v218\n"
      "v_cmp_lt_i32 s[12:13], s56, v220\n"
      "v_cmp_ge_i32 vcc, s57, v220\n"
      "s_nop 1\n"
      "s_and_b64 vcc, vcc, s[12:13]\n"
      "v_cndmask_b32 v202, v219, v202, vcc\n"
      "v_max_f32 v214, v214, v202\n"
      "v_sub_u32 v220, 9, v218\n"
      "v_cmp_lt_i32 s[12:13], s56, v220\n"
      "v_cmp_ge_i32 vcc, s57, v220\n"
      "s_nop 1\n"
      "s_and_b64 vcc, vcc, s[12:13]\n"
      "v_cndmask_b32 v203, v219, v203, vcc\n"
      "v_max_f32 v214, v214, v203\n"
      "v_sub_u32 v220, 10, v218\n"
      "v_cmp_lt_i32 s[12:13], s56, v220\n"
      "v_cmp_ge_i32 vcc, s57, v220\n"
      "s_nop 1\n"
      "s_and_b64 vcc, vcc, s[12:13]\n"
      "v_cndmask_b32 v204, v219, v204, vcc\n"
      "v_max_f32 v214, v214, v204\n"
      "v_sub_u32 v220, 11, v218\n"
      "v_cmp_lt_i32 s[12:13], s56, v220\n"
      "v_cmp_ge_i32 vcc, s57, v220\n"
      "s_nop 1\n"
      "s_and_b64 vcc, vcc, s[12:13]\n"
      "v_cndmask_b32 v205, v219, v205, vcc\n"
      "v_max_f32 v214, v214, v205\n"
      "v_sub_u32 v220, 16, v218\n"
      "v_cmp_lt_i32 s[12:13], s56, v220\n"
      "v_cmp_ge_i32 vcc, s57, v220\n"
      "s_nop 1\n"
      "s_and_b64 vcc, vcc, s[12:13]\n"
      "v_cndmask_b32 v206, v219, v206, vcc\n"
      "v_max_f32 v214, v214, v206\n"
      "v_sub_u32 v220, 17, v218\n"
      "v_cmp_lt_i32 s[12:13], s56, v220\n"
      "v_cmp_ge_i32 vcc, s57, v220\n"
      "s_nop 1\n"
      "s_and_b64 vcc, vcc, s[12:13]\n"
      "v_cndmask_b32 v207, v219, v207, vcc\n"
      "v_max_f32 v214, v214, v207\n"
      "v_sub_u32 v220, 18, v218\n"
      "v_cmp_lt_i32 s[12:13], s56, v220\n"
      "v_cmp_ge_i32 vcc, s57, v220\n"
      "s_nop 1\n"
      "s_and_b64 vcc, vcc, s[12:13]\n"
      "v_cndmask_b32 v208, v219, v208, vcc\n"
      "v_max_f32 v214, v214, v208\n"
      "v_sub_u32 v220, 19, v218\n"
      "v_cmp_lt_i32 s[12:13], s56, v220\n"
      "v_cmp_ge_i32 vcc, s57, v220\n"
      "s_nop 1\n"
      "s_and_b64 vcc, vcc, s[12:13]\n"
      "v_cndmask_b32 v209, v219, v209, vcc\n"
      "v_max_f32 v214, v214, v209\n"
      "v_sub_u32 v220, 24, v218\n"
      "v_cmp_lt_i32 s[12:13], s56, v220\n"
      "v_cmp_ge_i32 vcc, s57, v220\n"
      "s_nop 1\n"
      "s_and_b64 vcc, vcc, s[12:13]\n"
      "v_cndmask_b32 v210, v219, v210, vcc\n"
      "v_max_f32 v214, v214, v210\n"
      "v_sub_u32 v220, 25, v218\n"
      "v_cmp_lt_i32 s[12:13], s56, v220\n"
      "v_cmp_ge_i32 vcc, s57, v220\n"
      "s_nop 1\n"
      "s_and_b64 vcc, vcc, s[12:13]\n"
      "v_cndmask_b32 v211, v219, v211, vcc\n"
      "v_max_f32 v214, v214, v211\n"
      "v_sub_u32 v220, 26, v218\n"
      "v_cmp_lt_i32 s[12:13], s56, v220\n"
      "v_cmp_ge_i32 vcc, s57, v220\n"
      "s_nop 1\n"
      "s_and_b64 vcc, vcc, s[12:13]\n"
      "v_cndmask_b32 v212, v219, v212, vcc\n"
      "v_max_f32 v214, v214, v212\n"
      "v_sub_u32 v220, 27, v218\n"
      "v_cmp_lt_i32 s[12:13], s56, v220\n"
      "v_cmp_ge_i32 vcc, s57, v220\n"
      "s_nop 1\n"
      "s_and_b64 vcc, vcc, s[12:13]\n"
      "v_cndmask_b32 v213, v219, v213, vcc\n"
      "v_max_f32 v214, v214, v213\n"
      "v_subrev_u32 v215, 0x2000, v215\n"
      "v_sub_u32 v216, v216, v77\n"
      "s_add_u32 s58, s58, 1\n"
      "s_cmp_le_u32 s58, s59\n"
      "s_cbranch_scc1 .Lslowloop_2509_%=\n"
      "v_mov_b32 v220, v214\n"
      "s_nop 1\n"
      "v_permlane32_swap_b32 v214, v220\n"
      "v_max_f32 v214, v214, v220\n"
      "s_mov_b32 s58, 0\n"
      "v_add_u32 v215, 0, v74\n"
      "v_mov_b32 v216, v86\n"
      "v_mov_b32 v130, 0\n"
      "v_mov_b32 v131, 0\n"
      "v_mov_b32 v132, 0\n"
      "v_mov_b32 v133, 0\n"
      "v_mov_b32 v134, 0\n"
      "v_mov_b32 v135, 0\n"
      "v_mov_b32 v136, 0\n"
      "v_mov_b32 v137, 0\n"
      "v_mov_b32 v138, 0\n"
      "v_mov_b32 v139, 0\n"
      "v_mov_b32 v140, 0\n"
      "v_mov_b32 v141, 0\n"
      "v_mov_b32 v142, 0\n"
      "v_mov_b32 v143, 0\n"
      "v_mov_b32 v144, 0\n"
      "v_mov_b32 v145, 0\n"
      "s_nop 1\n"
      ".Lslowloop_2510_%=:\n"
      "ds_read_b128 v[222:225], v215 offset:0\n"
      "ds_read_b128 v[226:229], v216 offset:0\n"
      "ds_read_b128 v[230:233], v216 offset:1024\n"
      "s_cmp_eq_u32 s58, 0\n"
      "s_cselect_b32 s57, 0, 64\n"
      "s_cmp_eq_u32 s58, 8\n"
      "s_cselect_b32 s56, 0, -64\n"
      "s_waitcnt lgkmcnt(2)\n"
      "v_mfma_f32_32x32x16_bf16 v[198:213], v[222:225], v[8:11], 0\n"
      "v_sub_u32 v220, 0, v218\n"
      "v_cmp_lt_i32 s[12:13], s56, v220\n"
      "v_cmp_ge_i32 vcc, s57, v220\n"
      "s_nop 1\n"
      "s_and_b64 vcc, vcc, s[12:13]\n"
      "s_nop 5\n"
      "v_cndmask_b32 v198, v219, v198, vcc\n"
      "v_sub_f32 v198, v198, v214\n"
      "v_exp_f32 v198, v198\n"
      "s_nop 0\n"
      "v_cndmask_b32 v198, 0, v198, vcc\n"
      "v_sub_u32 v220, 1, v218\n"
      "v_cmp_lt_i32 s[12:13], s56, v220\n"
      "v_cmp_ge_i32 vcc, s57, v220\n"
      "s_nop 1\n"
      "s_and_b64 vcc, vcc, s[12:13]\n"
      "v_cndmask_b32 v199, v219, v199, vcc\n"
      "v_sub_f32 v199, v199, v214\n"
      "v_exp_f32 v199, v199\n"
      "s_nop 0\n"
      "v_cndmask_b32 v199, 0, v199, vcc\n"
      "v_sub_u32 v220, 2, v218\n"
      "v_cmp_lt_i32 s[12:13], s56, v220\n"
      "v_cmp_ge_i32 vcc, s57, v220\n"
      "s_nop 1\n"
      "s_and_b64 vcc, vcc, s[12:13]\n"
      "v_cndmask_b32 v200, v219, v200, vcc\n"
      "v_sub_f32 v200, v200, v214\n"
      "v_exp_f32 v200, v200\n"
      "s_nop 0\n"
      "v_cndmask_b32 v200, 0, v200, vcc\n"
      "v_sub_u32 v220, 3, v218\n"
      "v_cmp_lt_i32 s[12:13], s56, v220\n"
      "v_cmp_ge_i32 vcc, s57, v220\n"
      "s_nop 1\n"
      "s_and_b64 vcc, vcc, s[12:13]\n"
      "v_cndmask_b32 v201, v219, v201, vcc\n"
      "v_sub_f32 v201, v201, v214\n"
      "v_exp_f32 v201, v201\n"
      "s_nop 0\n"
      "v_cndmask_b32 v201, 0, v201, vcc\n"
      "v_sub_u32 v220, 8, v218\n"
      "v_cmp_lt_i32 s[12:13], s56, v220\n"
      "v_cmp_ge_i32 vcc, s57, v220\n"
      "s_nop 1\n"
      "s_and_b64 vcc, vcc, s[12:13]\n"
      "v_cndmask_b32 v202, v219, v202, vcc\n"
      "v_sub_f32 v202, v202, v214\n"
      "v_exp_f32 v202, v202\n"
      "s_nop 0\n"
      "v_cndmask_b32 v202, 0, v202, vcc\n"
      "v_sub_u32 v220, 9, v218\n"
      "v_cmp_lt_i32 s[12:13], s56, v220\n"
      "v_cmp_ge_i32 vcc, s57, v220\n"
      "s_nop 1\n"
      "s_and_b64 vcc, vcc, s[12:13]\n"
      "v_cndmask_b32 v203, v219, v203, vcc\n"
      "v_sub_f32 v203, v203, v214\n"
      "v_exp_f32 v203, v203\n"
      "s_nop 0\n"
      "v_cndmask_b32 v203, 0, v203, vcc\n"
      "v_sub_u32 v220, 10, v218\n"
      "v_cmp_lt_i32 s[12:13], s56, v220\n"
      "v_cmp_ge_i32 vcc, s57, v220\n"
      "s_nop 1\n"
      "s_and_b64 vcc, vcc, s[12:13]\n"
      "v_cndmask_b32 v204, v219, v204, vcc\n"
      "v_sub_f32 v204, v204, v214\n"
      "v_exp_f32 v204, v204\n"
      "s_nop 0\n"
      "v_cndmask_b32 v204, 0, v204, vcc\n"
      "v_sub_u32 v220, 11, v218\n"
      "v_cmp_lt_i32 s[12:13], s56, v220\n"
      "v_cmp_ge_i32 vcc, s57, v220\n"
      "s_nop 1\n"
      "s_and_b64 vcc, vcc, s[12:13]\n"
      "v_cndmask_b32 v205, v219, v205, vcc\n"
      "v_sub_f32 v205, v205, v214\n"
      "v_exp_f32 v205, v205\n"
      "s_nop 0\n"
      "v_cndmask_b32 v205, 0, v205, vcc\n"
      "v_sub_u32 v220, 16, v218\n"
      "v_cmp_lt_i32 s[12:13], s56, v220\n"
      "v_cmp_ge_i32 vcc, s57, v220\n"
      "s_nop 1\n"
      "s_and_b64 vcc, vcc, s[12:13]\n"
      "v_cndmask_b32 v206, v219, v206, vcc\n"
      "v_sub_f32 v206, v206, v214\n"
      "v_exp_f32 v206, v206\n"
      "s_nop 0\n"
      "v_cndmask_b32 v206, 0, v206, vcc\n"
      "v_sub_u32 v220, 17, v218\n"
      "v_cmp_lt_i32 s[12:13], s56, v220\n"
      "v_cmp_ge_i32 vcc, s57, v220\n"
      "s_nop 1\n"
      "s_and_b64 vcc, vcc, s[12:13]\n"
      "v_cndmask_b32 v207, v219, v207, vcc\n"
      "v_sub_f32 v207, v207, v214\n"
      "v_exp_f32 v207, v207\n"
      "s_nop 0\n"
      "v_cndmask_b32 v207, 0, v207, vcc\n"
      "v_sub_u32 v220, 18, v218\n"
      "v_cmp_lt_i32 s[12:13], s56, v220\n"
      "v_cmp_ge_i32 vcc, s57, v220\n"
      "s_nop 1\n"
      "s_and_b64 vcc, vcc, s[12:13]\n"
      "v_cndmask_b32 v208, v219, v208, vcc\n"
      "v_sub_f32 v208, v208, v214\n"
      "v_exp_f32 v208, v208\n"
      "s_nop 0\n"
      "v_cndmask_b32 v208, 0, v208, vcc\n"
      "v_sub_u32 v220, 19, v218\n"
      "v_cmp_lt_i32 s[12:13], s56, v220\n"
      "v_cmp_ge_i32 vcc, s57, v220\n"
      "s_nop 1\n"
      "s_and_b64 vcc, vcc, s[12:13]\n"
      "v_cndmask_b32 v209, v219, v209, vcc\n"
      "v_sub_f32 v209, v209, v214\n"
      "v_exp_f32 v209, v209\n"
      "s_nop 0\n"
      "v_cndmask_b32 v209, 0, v209, vcc\n"
      "v_sub_u32 v220, 24, v218\n"
      "v_cmp_lt_i32 s[12:13], s56, v220\n"
      "v_cmp_ge_i32 vcc, s57, v220\n"
      "s_nop 1\n"
      "s_and_b64 vcc, vcc, s[12:13]\n"
      "v_cndmask_b32 v210, v219, v210, vcc\n"
      "v_sub_f32 v210, v210, v214\n"
      "v_exp_f32 v210, v210\n"
      "s_nop 0\n"
      "v_cndmask_b32 v210, 0, v210, vcc\n"
      "v_sub_u32 v220, 25, v218\n"
      "v_cmp_lt_i32 s[12:13], s56, v220\n"
      "v_cmp_ge_i32 vcc, s57, v220\n"
      "s_nop 1\n"
      "s_and_b64 vcc, vcc, s[12:13]\n"
      "v_cndmask_b32 v211, v219, v211, vcc\n"
      "v_sub_f32 v211, v211, v214\n"
      "v_exp_f32 v211, v211\n"
      "s_nop 0\n"
      "v_cndmask_b32 v211, 0, v211, vcc\n"
      "v_sub_u32 v220, 26, v218\n"
      "v_cmp_lt_i32 s[12:13], s56, v220\n"
      "v_cmp_ge_i32 vcc, s57, v220\n"
      "s_nop 1\n"
      "s_and_b64 vcc, vcc, s[12:13]\n"
      "v_cndmask_b32 v212, v219, v212, vcc\n"
      "v_sub_f32 v212, v212, v214\n"
      "v_exp_f32 v212, v212\n"
      "s_nop 0\n"
      "v_cndmask_b32 v212, 0, v212, vcc\n"
      "v_sub_u32 v220, 27, v218\n"
      "v_cmp_lt_i32 s[12:13], s56, v220\n"
      "v_cmp_ge_i32 vcc, s57, v220\n"
      "s_nop 1\n"
      "s_and_b64 vcc, vcc, s[12:13]\n"
      "v_cndmask_b32 v213, v219, v213, vcc\n"
      "v_sub_f32 v213, v213, v214\n"
      "v_exp_f32 v213, v213\n"
      "s_nop 0\n"
      "v_cndmask_b32 v213, 0, v213, vcc\n"
      "v_cvt_pk_bf16_f32 v234, v198, v199\n"
      "v_cvt_pk_bf16_f32 v235, v200, v201\n"
      "v_cvt_pk_bf16_f32 v236, v202, v203\n"
      "v_cvt_pk_bf16_f32 v237, v204, v205\n"
      "v_cvt_pk_bf16_f32 v238, v206, v207\n"
      "v_cvt_pk_bf16_f32 v239, v208, v209\n"
      "v_cvt_pk_bf16_f32 v240, v210, v211\n"
      "v_cvt_pk_bf16_f32 v241, v212, v213\n"
      "s_waitcnt lgkmcnt(1)\n"
      "v_mfma_f32_32x32x16_bf16 v[130:145], v[226:229], v[234:237], v[130:145]\n"
      "s_waitcnt lgkmcnt(0)\n"
      "v_mfma_f32_32x32x16_bf16 v[130:145], v[230:233], v[238:241], v[130:145]\n"
      "v_subrev_u32 v215, 0x2000, v215\n"
      "v_sub_u32 v216, v216, v77\n"
      "s_add_u32 s58, s58, 1\n"
      "s_cmp_le_u32 s58, s59\n"
      "s_nop 7\n"
      "s_cbranch_scc1 .Lslowloop_2510_%=\n"
      "v_rcp_f32 v188, v138\n"
      "s_nop 1\n"
      "s_branch .Lback_2008_%=\n"
      :
      : "s"(__builtin_amdgcn_kernarg_segment_ptr()), "s"(blockIdx.x), "{v0}"(threadIdx.x), "s"((unsigned)(size_t)(__attribute__((address_space(3))) char*)smem)
      : "memory", "vcc", "scc", "v1", "v2", "v3", "v4", "v5", "v6", "v7", "v8", "v9", "v10", "v11", "v12", "v13", "v14", "v15", "v16", "v17", "v18", "v19", "v20", "v21", "v22", "v23", "v24", "v25", "v26", "v27", "v28", "v29", "v30", "v31", "v32", "v33", "v34", "v35", "v36", "v37", "v38", "v39", "v40", "v41", "v42", "v43", "v44", "v45", "v46", "v47", "v48", "v49", "v50", "v51", "v52", "v53", "v54", "v55", "v56", "v57", "v58", "v59", "v60", "v61", "v62", "v63", "v64", "v65", "v66", "v67", "v68", "v69", "v70", "v71", "v72", "v73", "v74", "v75", "v76", "v77", "v78", "v79", "v80", "v81", "v82", "v83", "v84", "v85", "v86", "v87", "v88", "v89", "v90", "v91", "v92", "v93", "v94", "v95", "v96", "v97", "v98", "v99", "v100", "v101", "v102", "v103", "v104", "v105", "v106", "v107", "v108", "v109", "v110", "v111", "v112", "v113", "v114", "v115", "v116", "v117", "v118", "v119", "v120", "v121", "v122", "v123", "v124", "v125", "v126", "v127", "v128", "v129", "v130", "v131", "v132", "v133", "v134", "v135", "v136", "v137", "v138", "v139", "v140", "v141", "v142", "v143", "v144", "v145", "v146", "v147", "v148", "v149", "v150", "v151", "v152", "v153", "v154", "v155", "v156", "v157", "v158", "v159", "v160", "v161", "v162", "v163", "v164", "v165", "v166", "v167", "v168", "v169", "v170", "v171", "v172", "v173", "v174", "v175", "v176", "v177", "v178", "v179", "v180", "v181", "v182", "v183", "v184", "v185", "v186", "v187", "v188", "v189", "v190", "v191", "v192", "v193", "v194", "v195", "v196", "v197", "v198", "v199", "v200", "v201", "v202", "v203", "v204", "v205", "v206", "v207", "v208", "v209", "v210", "v211", "v212", "v213", "v214", "v215", "v216", "v217", "v218", "v219", "v220", "v221", "v222", "v223", "v224", "v225", "v226", "v227", "v228", "v229", "v230", "v231", "v232", "v233", "v234", "v235", "v236", "v237", "v238", "v239", "v240", "v241", "v242", "v243", "v244", "v245", "v246", "v247", "v248", "v249", "v250", "v251", "v252", "v253", "v254", "v255", "s4", "s5", "s6", "s7", "s8", "s9", "s10", "s11", "s12", "s13", "s14", "s15", "s16", "s17", "s18", "s19", "s20", "s21", "s22", "s23", "s24", "s25", "s26", "s27", "s28", "s29", "s30", "s31", "s33", "s34", "s35", "s36", "s37", "s38", "s39", "s40", "s41", "s42", "s43", "s44", "s45", "s46", "s47", "s48", "s49", "s50", "s51", "s52", "s53", "s54", "s55", "s56", "s57", "s58", "s59", "s60", "s61", "s62", "s63", "s64", "s65", "s66", "s67", "s68", "s69", "s70", "s71", "s72", "s73", "s74", "s75", "s76", "s77", "s78", "s79", "s80", "s81", "s82", "s83", "s84", "s85", "s86", "s87", "s88", "s89", "s90", "s91", "s92", "s93", "s94", "s95", "s96", "s97", "s98", "s99");
}
}

extern "C" void kernel_launch(void* const* d_in, const int* in_sizes, int n_in, void* d_out, int out_size, void* d_ws,
                              size_t ws_size, hipStream_t stream) {
  fb::fused_block<<<256, 512, 0, stream>>>(d_in[0], d_in[1], d_in[2], d_in[4], d_in[5], d_in[6], d_in[7], d_out, d_ws);
}
```
